```python
import torch

def _softmax(x, axis=-1):
    return torch.softmax(x, dim=axis)
def _gelu(x):
    return torch.nn.functional.gelu(x, approximate="tanh")
def _silu(x):
    return torch.nn.functional.silu(x)
def _sigmoid(x):
    return torch.sigmoid(x)
def _layernorm(x, axis=-1, eps=1e-5):
    m = x.mean(dim=axis, keepdim=True)
    v = x.var(dim=axis, keepdim=True, unbiased=False)
    return (x - m) / torch.sqrt(v + eps)
def _scatter_add(tgt, idx, src, axis=0):
    return tgt.clone().index_add_(axis, idx, src)
def _segment_sum(src, seg, n):
    out = torch.zeros((n,) + src.shape[1:], dtype=src.dtype)
    return out.index_add_(0, seg, src)
def _pad(x, pw):
    flat = tuple(v for pair in reversed(pw) for v in pair)
    return torch.nn.functional.pad(x, flat)

kernel_name = 'concat_heads__ssm_conv_s5__ssm_conv_s4__prompt'

def setup_inputs():

    _w = (lambda a: (lambda t: t.to(torch.bfloat16) if t.is_floating_point() else t)(torch.as_tensor(a)))
    def _A_setup():
        B, S, D = 1, 2048, 512
        P = D
        Lambda_re = -0.5 * (lambda _x: torch.exp(torch.as_tensor(_x)))((lambda *s: torch.randn(*s, dtype=torch.bfloat16) * 0.2)(P))
        Lambda_im = (lambda *s: torch.randn(*s, dtype=torch.bfloat16) * 0.4)(P) * 1.0
        B_re = (lambda *s: torch.randn(*s, dtype=torch.bfloat16) * 0.2)(P, D) * (D ** -0.5)
        B_im = (lambda *s: torch.randn(*s, dtype=torch.bfloat16) * 0.2)(P, D) * (D ** -0.5)
        C_re = (lambda *s: torch.randn(*s, dtype=torch.bfloat16) * 0.2)(D, P) * (P ** -0.5)
        C_im = (lambda *s: torch.randn(*s, dtype=torch.bfloat16) * 0.2)(D, P) * (P ** -0.5)
        log_dt = (lambda *s: torch.randn(*s, dtype=torch.bfloat16) * 0.2)(P) * 0.3 - 2.0
        Dskip = (lambda *s: torch.randn(*s, dtype=torch.bfloat16) * 0.05)(D)
        return {
            "x": _w((lambda *s: torch.randn(*s, dtype=torch.bfloat16) * 0.05)(B, S, D)),
            "Lambda_re": _w(Lambda_re),
            "Lambda_im": _w(Lambda_im),
            "B_re": _w(B_re),
            "B_im": _w(B_im),
            "C_re": _w(C_re),
            "C_im": _w(C_im),
            "log_dt": _w(log_dt),
            "Dskip": _w(Dskip),
        }
    _A_in = _A_setup()

    def _B_setup():
        B, L, D, N = 1, 2048, 512, 128
        A_re = -(lambda _x: torch.exp(torch.as_tensor(_x)))((lambda *s: torch.randn(*s, dtype=torch.bfloat16) * 3.2)(D, N))
        A_im = torch.pi * (lambda *s: torch.randn(*s, dtype=torch.bfloat16) * 0.05)(D, N)
        B_re = (lambda *s: torch.randn(*s, dtype=torch.bfloat16) * 0.1)(D, N)
        B_im = (lambda *s: torch.randn(*s, dtype=torch.bfloat16) * 0.1)(D, N)
        C_re = (lambda *s: torch.randn(*s, dtype=torch.bfloat16) * 0.1)(D, N)
        C_im = (lambda *s: torch.randn(*s, dtype=torch.bfloat16) * 0.1)(D, N)
        log_dt = (lambda *s: torch.randn(*s, dtype=torch.bfloat16) * 0.05)(D) * 0.5 - 4.0
        Dskip = (lambda *s: torch.randn(*s, dtype=torch.bfloat16) * 0.05)(D)
        return {
            "u": _w((lambda *s: torch.randn(*s, dtype=torch.bfloat16) * 0.05)(B, L, D)),
            "A_re": _w(A_re),
            "A_im": _w(A_im),
            "B_re": _w(B_re),
            "B_im": _w(B_im),
            "C_re": _w(C_re),
            "C_im": _w(C_im),
            "log_dt": _w(log_dt),
            "Dskip": _w(Dskip),
        }
    _B_in = _B_setup()

    return {
        "A_x": _A_in["x"],
        "A_Lambda_re": _A_in["Lambda_re"],
        "A_Lambda_im": _A_in["Lambda_im"],
        "A_B_re": _A_in["B_re"],
        "A_B_im": _A_in["B_im"],
        "A_C_re": _A_in["C_re"],
        "A_C_im": _A_in["C_im"],
        "A_log_dt": _A_in["log_dt"],
        "A_Dskip": _A_in["Dskip"],
        "B_u": _B_in["u"],
        "B_A_re": _B_in["A_re"],
        "B_A_im": _B_in["A_im"],
        "B_B_re": _B_in["B_re"],
        "B_B_im": _B_in["B_im"],
        "B_C_re": _B_in["C_re"],
        "B_C_im": _B_in["C_im"],
        "B_log_dt": _B_in["log_dt"],
        "B_Dskip": _B_in["Dskip"],
    }

def reference(A_x, A_Lambda_re, A_Lambda_im, A_B_re, A_B_im, A_C_re, A_C_im, A_log_dt, A_Dskip, B_u, B_A_re, B_A_im, B_B_re, B_B_im, B_C_re, B_C_im, B_log_dt, B_Dskip):
    def _A_ref(x, Lambda_re, Lambda_im, B_re, B_im, C_re, C_im, log_dt, Dskip):
        Bsz, L, H = x.shape
        Lam = Lambda_re + 1j * Lambda_im
        Bmat = B_re + 1j * B_im
        Cmat = C_re + 1j * C_im
        dt = torch.exp(log_dt)
        Lam_bar = torch.exp(dt * Lam)
        B_bar = ((Lam_bar - 1.0) / Lam)[:, None] * Bmat
        Bu = torch.einsum("bld,pd->blp", x + 0j, B_bar)
        h = torch.zeros_like(Bu[:, 0, :])
        hs = []
        for t in range(L):
            h = Lam_bar[None, :] * h + Bu[:, t, :]
            hs.append(h)
        hseq = torch.stack(hs, 1)
        y = 2.0 * torch.einsum("blp,dp->bld", hseq, Cmat).real + x * Dskip[None, None, :]
        return y

    def _B_ref(u, A_re, A_im, B_re, B_im, C_re, C_im, log_dt, Dskip):
        Bsz, L, D = u.shape
        dt = torch.exp(log_dt)[:, None]
        A = A_re + 1j * A_im
        Bc = B_re + 1j * B_im
        Cc = C_re + 1j * C_im
        logdA = dt * A
        dB = dt * Bc
        l = (lambda x, dt: x.to(dt))(torch.arange(L), A_re.dtype)
        P = torch.exp(logdA[:, :, None] * l[None, None, :])
        K = torch.einsum("dn,dnl,dn->dl", Cc, P, dB).real
        L2 = 2 * L
        Kp = _pad(K, ((0, 0), (0, L)))
        up = (lambda x, *d: x.permute(*d))(u, 0, 2, 1)
        up = _pad(up, ((0, 0), (0, 0), (0, L)))
        Kf = (lambda x, axes=(-2,-1): torch.fft.rfft2(x.float(), dim=axes))(Kp[:, None, :], axes=(-2, -1))
        Uf = (lambda x, axes=(-2,-1): torch.fft.rfft2(x.float(), dim=axes))(up[:, :, None, :], axes=(-2, -1))
        Yf = Uf * Kf[None, :, :, :]
        y = (lambda x, s, axes=(-2,-1): torch.fft.irfft2(x, s=s, dim=axes).to(torch.bfloat16))(Yf, (1, L2), axes=(-2, -1))[:, :, 0, :L]
        y = (lambda x, *d: x.permute(*d))(y, 0, 2, 1) + u * Dskip[None, None, :]
        return y

    a = _A_ref(A_x, A_Lambda_re, A_Lambda_im, A_B_re, A_B_im, A_C_re, A_C_im, A_log_dt, A_Dskip)
    b = _B_ref(B_u, B_A_re, B_A_im, B_B_re, B_B_im, B_C_re, B_C_im, B_log_dt, B_Dskip)
    return torch.concatenate([a, b], axis=-1)
```

```cpp
#include <hip/hip_runtime.h>
#include <hip/hip_bf16.h>
#include <stdint.h>

#define ROUND_L 0
#define ROUND_DT 1

typedef unsigned short u16;
__device__ __forceinline__ float bf2f(u16 v){ return __uint_as_float(((unsigned)v)<<16); }
__device__ __forceinline__ float rbf(float x){
  unsigned u = __float_as_uint(x);
  u = (u + 0x7FFFu + ((u>>16)&1u)) & 0xFFFF0000u;
  return __uint_as_float(u);
}
constexpr int L=2048, D=512, P=512, N=128;

__global__ void kA1(const u16* x, const u16* Lre, const u16* Lim, const u16* Bre, const u16* Bim, const u16* logdt, float2* Bu){
  int p = blockIdx.x*blockDim.x+threadIdx.x; int l = blockIdx.y;
  float zr=0, zi=0;
  for(int d=0; d<D; ++d){ float xv=bf2f(x[l*D+d]); zr += xv*bf2f(Bre[p*D+d]); zi += xv*bf2f(Bim[p*D+d]); }
  float dt = expf(bf2f(logdt[p])); if(ROUND_DT) dt = rbf(dt);
  float lr=bf2f(Lre[p]), li=bf2f(Lim[p]);
  float e = expf(dt*lr); float s,c; sincosf(dt*li,&s,&c);
  float nr = e*c-1.f, ni = e*s;
  float den = lr*lr+li*li;
  float cr = (nr*lr+ni*li)/den, ci = (ni*lr-nr*li)/den;
  Bu[l*P+p] = make_float2(cr*zr-ci*zi, cr*zi+ci*zr);
}
__global__ void kA2(const u16* Lre, const u16* Lim, const u16* logdt, float2* Bu){
  int p = blockIdx.x*blockDim.x+threadIdx.x;
  float dt = expf(bf2f(logdt[p])); if(ROUND_DT) dt = rbf(dt);
  float lr=bf2f(Lre[p]), li=bf2f(Lim[p]);
  float e = expf(dt*lr); float s,c; sincosf(dt*li,&s,&c);
  float ar=e*c, ai=e*s; float hr=0, hi=0;
  for(int l=0;l<L;++l){ float2 b=Bu[l*P+p]; float nr=ar*hr-ai*hi+b.x, ni=ar*hi+ai*hr+b.y; hr=nr; hi=ni; Bu[l*P+p]=make_float2(hr,hi);}
}
__global__ void kA3(const u16* x, const u16* Cre, const u16* Cim, const u16* Dsk, const float2* h, float* out){
  int d = blockIdx.x*blockDim.x+threadIdx.x; int l = blockIdx.y;
  float acc=0;
  for(int p=0;p<P;++p){ float2 hv=h[l*P+p]; acc += hv.x*bf2f(Cre[d*P+p]) - hv.y*bf2f(Cim[d*P+p]); }
  float sk = bf2f(x[l*D+d])*bf2f(Dsk[d]); if(ROUND_DT) sk = rbf(sk);
  out[l*1024+d] = 2.f*acc + sk;
}
__global__ void kB1(const u16* Are, const u16* Aim, const u16* Bre, const u16* Bim, const u16* Cre, const u16* Cim, const u16* logdt, float* K){
  int l = blockIdx.x*blockDim.x+threadIdx.x; int d = blockIdx.y;
  float dt = expf(bf2f(logdt[d])); if(ROUND_DT) dt = rbf(dt);
  float lf = (float)l; if(ROUND_L) lf = rbf(lf);
  float acc=0;
  for(int n=0;n<N;++n){
    float zr = dt*bf2f(Are[d*N+n]), zi = dt*bf2f(Aim[d*N+n]);
    float br = dt*bf2f(Bre[d*N+n]), bi = dt*bf2f(Bim[d*N+n]);
    float cr = bf2f(Cre[d*N+n]), ci = bf2f(Cim[d*N+n]);
    float wr = cr*br-ci*bi, wi = cr*bi+ci*br;
    float e = expf(zr*lf); float s,c; sincosf(zi*lf,&s,&c);
    acc += e*(wr*c - wi*s);
  }
  K[d*L+l]=acc;
}
__global__ void kB2(const u16* u, const u16* Dsk, const float* K, float* out){
  int d = blockIdx.x*blockDim.x+threadIdx.x; int l = blockIdx.y;
  float acc=0;
  for(int j=0;j<=l;++j) acc += K[d*L+j]*bf2f(u[(l-j)*D+d]);
  float sk = bf2f(u[l*D+d])*bf2f(Dsk[d]);
  float r;
  if(ROUND_DT) r = rbf(rbf(acc)+rbf(sk)); else r = rbf(acc)+sk;
  out[l*1024+512+d] = r;
}

extern "C" void kernel_launch(void* const* d_in, const int* in_sizes, int n_in,
                              void* d_out, int out_size, void* d_ws, size_t ws_size,
                              hipStream_t stream) {
  const u16* const* in = (const u16* const*)d_in;
  float* out = (float*)d_out;
  float2* Bu = (float2*)d_ws;
  float* K = (float*)((char*)d_ws + (size_t)L*P*8);
  kA1<<<dim3(P/64,L),64,0,stream>>>(in[0],in[1],in[2],in[3],in[4],in[7],Bu);
  kA2<<<P/64,64,0,stream>>>(in[1],in[2],in[7],Bu);
  kA3<<<dim3(D/64,L),64,0,stream>>>(in[0],in[5],in[6],in[8],Bu,out);
  kB1<<<dim3(L/64,D),64,0,stream>>>(in[10],in[11],in[12],in[13],in[14],in[15],in[16],K);
  kB2<<<dim3(D/64,L),64,0,stream>>>(in[9],in[17],K,out);
}
```

```cpp
#include <hip/hip_runtime.h>
#include <hip/hip_bf16.h>
#include <stdint.h>

typedef unsigned short u16;
typedef unsigned u32;
typedef unsigned long long u64;
typedef short bf16x8 __attribute__((ext_vector_type(8)));
typedef short s16x4 __attribute__((ext_vector_type(4)));
typedef float f32x4 __attribute__((ext_vector_type(4)));
typedef float f32x2 __attribute__((ext_vector_type(2)));
typedef unsigned u32x4 __attribute__((ext_vector_type(4)));
typedef unsigned u32x2 __attribute__((ext_vector_type(2)));
#define GAS __attribute__((address_space(1)))
#define LAS __attribute__((address_space(3)))
#define CAS __attribute__((address_space(4)))
#define DEV __device__ __forceinline__

constexpr int L = 2048, D = 512, P = 512, N = 128;
constexpr u32 H_OFF = 0;
constexpr u32 UT_OFF = 4u << 20;
constexpr u32 YT_OFF = 6u << 20;
constexpr u32 EG_OFF = 8u << 20;
constexpr u32 FH_OFF = EG_OFF + (128u << 10);
constexpr u32 FU_OFF = FH_OFF + 2048;
constexpr u32 FY_OFF = FU_OFF + 2048;
constexpr u32 EPA_OFF = FY_OFF + 2048;
constexpr u32 EPB_OFF = EPA_OFF + 128;
constexpr u32 WS_USED = EPB_OFF + 2048;
constexpr u32 DBG_OFF = 16u << 20;
constexpr int LDS_BYTES = 81920;
constexpr u32 SPIN_CAP = 1u << 20;
constexpr int AUX_SC1 = 16;

#define WAIT_V(n) asm volatile("s_waitcnt vmcnt(" #n ")" ::: "memory")
#define WAIT_L0() asm volatile("s_waitcnt lgkmcnt(0)" ::: "memory")
#define BAR() __builtin_amdgcn_s_barrier()
#define LBAR() do { WAIT_L0(); BAR(); asm volatile("" ::: "memory"); } while (0)

#define ST(k) do {} while (0)

DEV float bf2f(u32 v) { return __uint_as_float(v << 16); }
DEV float bfhi(u32 v) { return __uint_as_float(v & 0xffff0000u); }
DEV u32 pk2(float lo, float hi) {
  typedef __bf16 bf2 __attribute__((ext_vector_type(2)));
  bf2 r = __builtin_convertvector(f32x2{lo, hi}, bf2);
  return __builtin_bit_cast(u32, r);
}
DEV float rbf(float x) { return bf2f(pk2(x, x) & 0xffffu); }
DEV float fma1(float a, float b, float c) { float r; asm("v_fma_f32 %0, %1, %2, %3" : "=v"(r) : "v"(a), "v"(b), "v"(c)); return r; }
DEV float fnma1(float a, float b, float c) { float r; asm("v_fma_f32 %0, -%1, %2, %3" : "=v"(r) : "v"(a), "v"(b), "v"(c)); return r; }
DEV f32x2 cmul(f32x2 a, f32x2 b) {
  f32x2 t = f32x2{a.x, a.x} * b;
  return f32x2{fnma1(a.y, b.y, t.x), fma1(a.y, b.x, t.y)};
}
DEV f32x2 cfma(f32x2 a, f32x2 b, f32x2 c) {
  f32x2 t = __builtin_elementwise_fma(f32x2{a.x, a.x}, b, c);
  return f32x2{fnma1(a.y, b.y, t.x), fma1(a.y, b.x, t.y)};
}
DEV f32x2 cpow(f32x2 z, float k) {
  float e = __builtin_amdgcn_exp2f(z.x * k);
  float th = __builtin_amdgcn_fractf(z.y * k);
  return f32x2{e * __builtin_amdgcn_cosf(th), e * __builtin_amdgcn_sinf(th)};
}
DEV void glds16(const void* g, char* l) {
  __builtin_amdgcn_global_load_lds((const GAS void*)g, (LAS void*)l, 16, 0, 0);
}
DEV u32 next_tag(u32 e) { return e * 0x9E3779B1u + 0x7F4A7C15u; }
DEV f32x4 mfma16(bf16x8 a, bf16x8 b, f32x4 c) { return __builtin_amdgcn_mfma_f32_16x16x32_bf16(a, b, c, 0, 0, 0); }

struct Params {
  const u16 *x, *Lre, *Lim, *ABre, *ABim, *ACre, *ACim, *Alogdt, *AD;
  const u16 *u, *Are, *Aim, *BBre, *BBim, *BCre, *BCim, *Blogdt, *BD;
  float* out;
  char* ws;
};

DEV void poll_flags(__amdgpu_buffer_rsrc_t rs, u32 off, int cnt, u32 tag, int lane) {
  const u32 o = off + (u32)(lane & (cnt - 1)) * 4u;
#pragma unroll 1
  for (u32 spins = 0; spins < SPIN_CAP; ++spins) {
    u32 v = __builtin_amdgcn_raw_buffer_load_b32(rs, o, 0, AUX_SC1);
    if (__all(v == tag)) break;
    __builtin_amdgcn_s_sleep(1);
  }
  asm volatile("" ::: "memory");
}

constexpr int A_SLOT = 24576;
constexpr int A_TR = 65536;
constexpr int A_PAR = 75776;
constexpr int A_S = 76800;
constexpr int A_WT = 80896;

DEV void partA(const Params& pr, char* smem) {
  char* ws = pr.ws;
  const int b = blockIdx.x;
  const int xcd = b & 7, jj = b >> 3;
  const int r = 2 * xcd + (jj >> 4), s = jj & 15;
  const int tid = threadIdx.x, lane = tid & 63;
  const int w = __builtin_amdgcn_readfirstlane(tid >> 6);
  const int c = lane & 15, g = lane >> 4;
  __builtin_amdgcn_s_setprio(2);
  ST(0);
  const __amdgpu_buffer_rsrc_t rs = __builtin_amdgcn_make_buffer_rsrc(ws, 0, WS_USED, 0x00020000);

  {
    u64 p0 = (u64)pr.Lre, p1 = (u64)pr.Lim, p2 = (u64)pr.Alogdt;
    asm volatile("" : "+s"(p0), "+s"(p1), "+s"(p2));
    const u16* src = (const u16*)((g == 0) ? p0 : (g == 1) ? p1 : p2);
    __builtin_amdgcn_global_load_lds((const GAS void*)(src + 32 * s + 2 * c), (LAS void*)(smem + A_PAR + w * 256), 4, 0, 0);
  }
  const int lr = lane >> 3;
  const int sw = ((lane & 7) ^ lr) * 16;
  const char* xsrc = (const char*)pr.x + (size_t)(128 * r + 32 * w + 8 * (lr >> 2) + (lr & 3)) * 1024 + sw;
  const char* bsrc = (const char*)((w < 2) ? pr.ABre : pr.ABim) + (size_t)(32 * s + 16 * (w & 1) + lr) * 1024 + sw;
  const int rot = (s + r) & 7;
  auto stage_x = [&](int it) {
    const int kc = ((it + rot) & 7) * 128;
    char* dst = smem + (it % 3) * A_SLOT + w * 4096;
    glds16(xsrc + kc, dst);
    glds16(xsrc + kc + 16 * 1024, dst + 1024);
    glds16(xsrc + kc + 4 * 1024, dst + 2048);
    glds16(xsrc + kc + 20 * 1024, dst + 3072);
  };
  auto stage_b = [&](int it) {
    const int kc = ((it + rot) & 7) * 128;
    char* dst = smem + (it % 3) * A_SLOT + 16384 + w * 2048;
    glds16(bsrc + kc, dst);
    glds16(bsrc + kc + 8 * 1024, dst + 1024);
  };
  stage_x(0); stage_b(0);
  stage_x(1); stage_b(1);
  stage_x(2); stage_b(2);

  const u32 tag = next_tag(*(const CAS u32*)(ws + EPA_OFF));
  asm volatile("" ::"s"(tag));

  WAIT_V(18);
  f32x2 z[2], lam[2], coef[2];
#pragma unroll
  for (int pt = 0; pt < 2; ++pt) {
    const u16* pp = (const u16*)(smem + A_PAR + w * 256);
    float lre = bf2f(pp[16 * pt + c]), lim = bf2f(pp[32 + 16 * pt + c]), ldt = bf2f(pp[64 + 16 * pt + c]);
    float dt = rbf(expf(ldt));
    float a = dt * lre, th = dt * lim;
    z[pt] = f32x2{a * 1.4426950408889634f, th * 0.15915494309189535f};
    lam[pt] = cpow(z[pt], 1.0f);
    float fr = __builtin_amdgcn_fractf(z[pt].y);
    float sh = __builtin_amdgcn_sinf(__builtin_amdgcn_fractf(0.5f * z[pt].y));
    float nr = expm1f(a) * __builtin_amdgcn_cosf(fr) - 2.0f * sh * sh, ni = lam[pt].y;
    float inv = 2.0f / (lre * lre + lim * lim);
    coef[pt] = f32x2{(nr * lre + ni * lim) * inv, (ni * lre - nr * lim) * inv};
  }

  f32x4 acc[2][4];
#pragma unroll
  for (int i = 0; i < 2; ++i)
#pragma unroll
    for (int n = 0; n < 4; ++n) acc[i][n] = f32x4{0, 0, 0, 0};
  const int fro = c * 128;
  const int fs0 = ((g) ^ (c & 7)) * 16, fs1 = ((4 + g) ^ (c & 7)) * 16;
#pragma unroll
  for (int it = 0; it < 8; ++it) {
    if (it < 6) WAIT_V(12); else if (it == 6) WAIT_V(6); else WAIT_V(0);
    BAR();
    asm volatile("" ::: "memory");
    const char* sl = smem + (it % 3) * A_SLOT;
    bf16x8 af[2][2], bf[4][2];
#pragma unroll
    for (int i = 0; i < 2; ++i) {
      af[i][0] = *(const bf16x8*)(sl + w * 4096 + i * 2048 + fro + fs0);
      af[i][1] = *(const bf16x8*)(sl + w * 4096 + i * 2048 + fro + fs1);
    }
#pragma unroll
    for (int n = 0; n < 4; ++n) {
      bf[n][0] = *(const bf16x8*)(sl + 16384 + n * 2048 + fro + fs0);
      bf[n][1] = *(const bf16x8*)(sl + 16384 + n * 2048 + fro + fs1);
    }
    WAIT_L0();
    __builtin_amdgcn_sched_barrier(0);
    if (it + 3 < 8) stage_x(it + 3);
    BAR();
    asm volatile("" ::: "memory");
    if (it + 3 < 8) stage_b(it + 3);
    __builtin_amdgcn_sched_barrier(0);
#pragma unroll
    for (int k = 0; k < 2; ++k)
#pragma unroll
      for (int i = 0; i < 2; ++i)
#pragma unroll
        for (int n = 0; n < 4; ++n) acc[i][n] = mfma16(af[i][k], bf[n][k], acc[i][n]);
  }
  ST(1);

  const int rh = s >> 3, ct = s & 7;
  char* wreg = smem + w * 16384;
  auto stage_c = [&](const u16* Cm) {
    const char* base = (const char*)Cm + (size_t)(64 * ct) * 1024 + w * 256;
#pragma unroll
    for (int q = 0; q < 16; ++q) {
      const int cc = 4 * (q & 3) + g;
      glds16(base + (size_t)(4 * cc + (q >> 2)) * 1024 + ((c ^ cc) * 16), wreg + q * 1024);
    }
  };
  stage_c(pr.ACre);

  f32x2 hs[2][8];
#pragma unroll
  for (int pt = 0; pt < 2; ++pt) {
    f32x2 st = f32x2{0.f, 0.f};
#pragma unroll
    for (int j = 0; j < 8; ++j) {
      f32x2 zz = f32x2{acc[j >> 2][pt][j & 3], acc[j >> 2][2 + pt][j & 3]};
      st = (j == 0) ? zz : cfma(lam[pt], st, zz);
      hs[pt][j] = st;
    }
  }
  f32x2* Sm = (f32x2*)(smem + A_S);
  f32x2* Wt = (f32x2*)(smem + A_WT);
#pragma unroll
  for (int pt = 0; pt < 2; ++pt) {
    Sm[(4 * w + g) * 32 + 16 * pt + c] = hs[pt][7];
    f32x2 t = cmul(cpow(z[pt], (float)(8 * (3 - g))), hs[pt][7]);
    t.x += __shfl_xor(t.x, 16); t.y += __shfl_xor(t.y, 16);
    t.x += __shfl_xor(t.x, 32); t.y += __shfl_xor(t.y, 32);
    if (g == 0) Wt[w * 32 + 16 * pt + c] = t;
  }
  LBAR();
  const f32x2 zq = (g & 1) ? z[1] : z[0];
  if (w == 0 && r < 15) {
    const f32x2 l32 = cpow(zq, 32.0f);
    const int p = lane & 31;
    f32x2 e = Wt[p];
    e = cfma(l32, e, Wt[32 + p]);
    e = cfma(l32, e, Wt[64 + p]);
    e = cfma(l32, e, Wt[96 + p]);
    if (lane < 32) {
      u32x4 v = u32x4{__float_as_uint(e.x), tag, __float_as_uint(e.y), tag};
      __builtin_amdgcn_raw_buffer_store_b128(v, rs, EG_OFF + (u32)((s * 16 + r) * 32 + p) * 16u, 0, AUX_SC1);
    }
  }
  ST(2);
  f32x2 lc[2], pw[2], Q[2][8];
#pragma unroll
  for (int pt = 0; pt < 2; ++pt) {
    const f32x2 l8 = cpow(z[pt], 8.0f), l32 = cpow(z[pt], 32.0f);
    f32x2 ci = f32x2{0.f, 0.f};
#pragma unroll
    for (int gg = 0; gg < 3; ++gg) {
      f32x2 t = cfma(l8, ci, Sm[(4 * w + gg) * 32 + 16 * pt + c]);
      ci = (gg < g) ? t : ci;
    }
    f32x2 cw = f32x2{0.f, 0.f};
#pragma unroll
    for (int ww = 0; ww < 3; ++ww) {
      f32x2 t = cfma(l32, cw, Wt[ww * 32 + 16 * pt + c]);
      cw = (ww < w) ? t : cw;
    }
    lc[pt] = cfma(cpow(z[pt], (float)(8 * g)), cw, ci);
    pw[pt] = cpow(z[pt], (float)(32 * w + 8 * g));
    Q[pt][0] = cmul(lam[pt], coef[pt]);
#pragma unroll
    for (int j = 1; j < 8; ++j) Q[pt][j] = cmul(lam[pt], Q[pt][j - 1]);
#pragma unroll
    for (int j = 0; j < 8; ++j) hs[pt][j] = cmul(coef[pt], hs[pt][j]);
  }
  const int rot2 = ct & 3;
  bf16x8 cre[4][4];
  WAIT_V(0);
#pragma unroll
  for (int q = 0; q < 4; ++q) {
    const int sg = (q + rot2) & 3;
#pragma unroll
    for (int jt = 0; jt < 4; ++jt) cre[q][jt] = *(const bf16x8*)(wreg + (16 * jt + c) * 256 + (((4 * sg + g) ^ c) * 16));
  }
  LBAR();
  {
    const int p = lane & 31, hf = lane >> 5;
    const int r0 = 4 * w + hf, r1 = r0 + 2;
    const bool v0 = r0 < r, v1 = r1 < r;
    f32x2 hp = f32x2{0.f, 0.f};
    if (4 * w < r) {
      const f32x2 w0 = cpow(zq, (float)(128 * (r - 1 - r0))), w1 = cpow(zq, (float)(128 * (r - 1 - r1)));
      const u32 o0 = EG_OFF + (u32)((s * 16 + r0) * 32 + p) * 16u, o1 = o0 + 1024u;
      u32x4 a0, a1;
#pragma unroll 1
      for (u32 spins = 0; spins < SPIN_CAP; ++spins) {
        a0 = __builtin_amdgcn_raw_buffer_load_b128(rs, o0, 0, AUX_SC1);
        a1 = __builtin_amdgcn_raw_buffer_load_b128(rs, o1, 0, AUX_SC1);
        const bool ok = (!v0 || (a0.y == tag && a0.w == tag)) && (!v1 || (a1.y == tag && a1.w == tag));
        if (__all(ok)) break;
        __builtin_amdgcn_s_sleep(1);
      }
      f32x2 t0 = cmul(w0, f32x2{__uint_as_float(a0.x), __uint_as_float(a0.z)});
      f32x2 t1 = cmul(w1, f32x2{__uint_as_float(a1.x), __uint_as_float(a1.z)});
      hp = (v0 ? t0 : f32x2{0.f, 0.f}) + (v1 ? t1 : f32x2{0.f, 0.f});
    }
    stage_c(pr.ACim);
    hp.x += __shfl_xor(hp.x, 32); hp.y += __shfl_xor(hp.y, 32);
    if (lane < 32) Wt[w * 32 + p] = hp;
  }
  LBAR();
  ST(3);
  {
    char* tr = smem + A_TR + w * 2560;
    u32x4 pl[2][2];
#pragma unroll
    for (int pt = 0; pt < 2; ++pt) {
      const int p = 16 * pt + c;
      f32x2 H = (Wt[p] + Wt[32 + p]) + (Wt[64 + p] + Wt[96 + p]);
      f32x2 cy = cfma(pw[pt], H, lc[pt]);
      f32x2 h[8];
#pragma unroll
      for (int j = 0; j < 8; ++j) h[j] = cfma(Q[pt][j], cy, hs[pt][j]);
      pl[0][pt] = u32x4{pk2(h[0].x, h[1].x), pk2(h[2].x, h[3].x), pk2(h[4].x, h[5].x), pk2(h[6].x, h[7].x)};
      pl[1][pt] = u32x4{pk2(-h[0].y, -h[1].y), pk2(-h[2].y, -h[3].y), pk2(-h[4].y, -h[5].y), pk2(-h[6].y, -h[7].y)};
    }
#pragma unroll
    for (int pp = 0; pp < 2; ++pp) {
      *(u32x4*)(tr + c * 80 + g * 16) = pl[pp][0];
      *(u32x4*)(tr + (16 + c) * 80 + g * 16) = pl[pp][1];
      asm volatile("" ::: "memory");
#pragma unroll
      for (int tt = 0; tt < 2; ++tt) {
        const char* ra = tr + (8 * g + (c >> 2)) * 80 + (16 * tt + 4 * (c & 3)) * 2;
        s16x4 lo = __builtin_amdgcn_ds_read_tr16_b64_v4i16((LAS s16x4*)ra);
        s16x4 hi = __builtin_amdgcn_ds_read_tr16_b64_v4i16((LAS s16x4*)(ra + 4 * 80));
        u32x2 l2 = __builtin_bit_cast(u32x2, lo), h2 = __builtin_bit_cast(u32x2, hi);
        const u32 T16 = 8 * r + 2 * w + tt;
        __builtin_amdgcn_raw_buffer_store_b128(u32x4{l2.x, l2.y, h2.x, h2.y}, rs,
                                               H_OFF + ((T16 * 16 + s) * 2 + pp) * 1024u + lane * 16u, 0, AUX_SC1);
      }
      asm volatile("" ::: "memory");
    }
  }
  WAIT_V(0);
  LBAR();
  if (tid == 0) __builtin_amdgcn_raw_buffer_store_b32(tag, rs, FH_OFF + r * 128 + s * 4, 0, AUX_SC1);
  ST(4);

  poll_flags(rs, FH_OFF + r * 128 + w * 16, 4, tag, lane);
  ST(5);
  f32x4 y[4][4];
#pragma unroll
  for (int i = 0; i < 4; ++i)
#pragma unroll
    for (int j = 0; j < 4; ++j) y[i][j] = f32x4{0, 0, 0, 0};
  {
    const u32 hb = H_OFF + (u32)(8 * r + 4 * rh) * 32768u + lane * 16u;
    u32x4 hf[4][2][4];
#pragma unroll
    for (int q = 0; q < 4; ++q) {
      const u32 so = (u32)(4 * w + ((q + rot2) & 3)) * 2048u;
#pragma unroll
      for (int pp = 0; pp < 2; ++pp)
#pragma unroll
        for (int i = 0; i < 4; ++i)
          hf[q][pp][i] = __builtin_amdgcn_raw_buffer_load_b128(rs, hb + i * 32768u + pp * 1024u, so, AUX_SC1);
    }
#pragma unroll
    for (int q = 0; q < 4; ++q) {
      const int sg = (q + rot2) & 3;
      bf16x8 cim[4];
#pragma unroll
      for (int jt = 0; jt < 4; ++jt) cim[jt] = *(const bf16x8*)(wreg + (16 * jt + c) * 256 + (((4 * sg + g) ^ c) * 16));
#pragma unroll
      for (int i = 0; i < 4; ++i)
#pragma unroll
        for (int jt = 0; jt < 4; ++jt) y[i][jt] = mfma16(__builtin_bit_cast(bf16x8, hf[q][0][i]), cre[q][jt], y[i][jt]);
#pragma unroll
      for (int i = 0; i < 4; ++i)
#pragma unroll
        for (int jt = 0; jt < 4; ++jt) y[i][jt] = mfma16(__builtin_bit_cast(bf16x8, hf[q][1][i]), cim[jt], y[i][jt]);
    }
  }
  ST(6);
  const int row0 = 128 * r + 64 * rh + 16 * w + 4 * g, col0 = 64 * ct + 4 * c;
  u32x2 xs[4];
#pragma unroll
  for (int k = 0; k < 4; ++k) xs[k] = *(const u32x2*)(pr.x + (size_t)(row0 + k) * 512 + col0);
  const u32x2 dsk = *(const u32x2*)(pr.AD + col0);
  asm volatile("" ::: "memory");
#pragma unroll
  for (int i = 0; i < 4; ++i)
#pragma unroll
    for (int jt = 0; jt < 4; ++jt) *(f32x4*)(wreg + (i * 4 + jt) * 1024 + lane * 16) = y[i][jt];
  LBAR();
  f32x4 o[4];
#pragma unroll
  for (int jt = 0; jt < 4; ++jt) {
    const char* pb = smem + (w * 4 + jt) * 1024 + lane * 16;
    o[jt] = (*(const f32x4*)(pb) + *(const f32x4*)(pb + 16384)) + (*(const f32x4*)(pb + 32768) + *(const f32x4*)(pb + 49152));
  }
  const float d0 = bf2f(dsk.x), d1 = bfhi(dsk.x), d2 = bf2f(dsk.y), d3 = bfhi(dsk.y);
#pragma unroll
  for (int k = 0; k < 4; ++k) {
    const u32 s01 = pk2(bf2f(xs[k].x) * d0, bfhi(xs[k].x) * d1), s23 = pk2(bf2f(xs[k].y) * d2, bfhi(xs[k].y) * d3);
    f32x4 v = f32x4{o[0][k] + bf2f(s01), o[1][k] + bfhi(s01), o[2][k] + bf2f(s23), o[3][k] + bfhi(s23)};
    *(f32x4*)(pr.out + (size_t)(row0 + k) * 1024 + col0) = v;
  }
  if (r == 15 && s == 0 && tid == 0) __builtin_amdgcn_raw_buffer_store_b32(tag, rs, EPA_OFF, 0, AUX_SC1);
  ST(7);
}

constexpr int B_CS = 4160;
constexpr int B_R = 0;
constexpr int B_U = 33280;
constexpr int B_TAB = 42496;
constexpr int B_PAR = 58880;
constexpr int B_T = 71168;
constexpr int B_TS = 272;

DEV void partB(const Params& pr, char* smem) {
  char* ws = pr.ws;
  const int bb = blockIdx.x - 256;
  const int xcd = bb & 7, jj = bb >> 3;
  const int kap = 2 * xcd + (jj >> 4), mi = jj & 15;
  const int tid = threadIdx.x, lane = tid & 63;
  const int w = __builtin_amdgcn_readfirstlane(tid >> 6);
  const int c = lane & 15, g = lane >> 4;
  const int ch = w >> 1, half = w & 1;
  const int d = 32 * kap + 2 * mi + ch;
  ST(0);
  const __amdgpu_buffer_rsrc_t rs = __builtin_amdgcn_make_buffer_rsrc(ws, 0, WS_USED, 0x00020000);

  const int cq = tid & 7, rp = tid >> 3;
  u32x2 ut[4];
#pragma unroll
  for (int k = 0; k < 4; ++k) ut[k] = *(const u32x2*)(pr.u + (size_t)(128 * mi + 4 * rp + k) * 512 + 32 * kap + 4 * cq);
  const int n = tid & 127;
  const u32 rAre = pr.Are[d * N + n], rAim = pr.Aim[d * N + n], rBre = pr.BBre[d * N + n], rBim = pr.BBim[d * N + n];
  const u32 rCre = pr.BCre[d * N + n], rCim = pr.BCim[d * N + n];
  const float dt = rbf(expf(bf2f(pr.Blogdt[d])));
  const u32 tag = next_tag(*(const CAS u32*)(ws + EPB_OFF + kap * 128));
  asm volatile("" ::"s"(tag));

  if (tid < 64) *(u32x4*)(smem + B_U + (tid >> 5) * 4608 + (tid & 31) * 16) = u32x4{0, 0, 0, 0};
  if (tid >= 64 && tid < 80) *(u32x4*)(smem + B_R + ((tid - 64) >> 1) * B_CS + 4096 + (tid & 1) * 16) = u32x4{0, 0, 0, 0};

  {
    char* tb = smem + B_T + (4 * cq) * B_TS + rp * 8;
    *(u32x2*)(tb) = u32x2{__builtin_amdgcn_perm(ut[1].x, ut[0].x, 0x05040100u), __builtin_amdgcn_perm(ut[3].x, ut[2].x, 0x05040100u)};
    *(u32x2*)(tb + B_TS) = u32x2{__builtin_amdgcn_perm(ut[1].x, ut[0].x, 0x07060302u), __builtin_amdgcn_perm(ut[3].x, ut[2].x, 0x07060302u)};
    *(u32x2*)(tb + 2 * B_TS) = u32x2{__builtin_amdgcn_perm(ut[1].y, ut[0].y, 0x05040100u), __builtin_amdgcn_perm(ut[3].y, ut[2].y, 0x05040100u)};
    *(u32x2*)(tb + 3 * B_TS) = u32x2{__builtin_amdgcn_perm(ut[1].y, ut[0].y, 0x07060302u), __builtin_amdgcn_perm(ut[3].y, ut[2].y, 0x07060302u)};
  }
  {
    const f32x2 zz = f32x2{dt * bf2f(rAre) * 1.4426950408889634f, dt * bf2f(rAim) * 0.15915494309189535f};
    const f32x2 W = cmul(f32x2{bf2f(rCre), bf2f(rCim)}, f32x2{dt * bf2f(rBre), dt * bf2f(rBim)});
    const f32x2 W1 = cmul(W, cpow(zz, 256.f)), W2 = cmul(W, cpow(zz, 512.f)), W3 = cmul(W, cpow(zz, 1536.f));
    char* pp = smem + B_PAR + tid * 48;
    *(f32x4*)(pp) = f32x4{zz.x, zz.y, W.x, W.y};
    *(f32x4*)(pp + 16) = f32x4{W1.x, W1.y, W2.x, W2.y};
    *(f32x2*)(pp + 32) = W3;
  }
  LBAR();
  {
#pragma unroll
    for (int k = 0; k < 2; ++k) {
      const int row = 16 * k + (tid >> 4);
      u32x4 v = *(const u32x4*)(smem + B_T + row * B_TS + c * 16);
      __builtin_amdgcn_raw_buffer_store_b128(v, rs, UT_OFF + (u32)(32 * kap + row) * 4096u + mi * 256u + c * 16u, 0, AUX_SC1);
    }
    WAIT_V(0);
    LBAR();
    if (tid == 0) __builtin_amdgcn_raw_buffer_store_b32(tag, rs, FU_OFF + kap * 128 + mi * 4, 0, AUX_SC1);
  }
  ST(1);
  {
    f32x4 T[4];
#pragma unroll
    for (int k = 0; k < 4; ++k) T[k] = f32x4{0, 0, 0, 0};
    const float fi = (float)c;
#pragma unroll
    for (int st = 0; st < 4; ++st) {
      u32x4 F0, F2, Ga, Gb, Gc, Gd;
#pragma unroll
      for (int mm = 0; mm < 4; ++mm) {
        const char* pp = smem + B_PAR + (w * 64 + st * 16 + 4 * g + mm) * 48;
        const f32x4 a = *(const f32x4*)pp, bq = *(const f32x4*)(pp + 16);
        const f32x2 w3 = *(const f32x2*)(pp + 32);
        f32x2 f = cpow(f32x2{a.x, a.y}, fi);
        F0[mm] = pk2(f.x, f.y);
        f = cmul(f, f); f = cmul(f, f);
        F2[mm] = pk2(f.x, f.y);
        f = cmul(f, f); f = cmul(f, f);
        f32x2 t = cmul(f32x2{a.z, a.w}, f); Ga[mm] = pk2(t.x, -t.y);
        t = cmul(f32x2{bq.x, bq.y}, f); Gb[mm] = pk2(t.x, -t.y);
        f = cmul(f, f); f = cmul(f, f);
        t = cmul(f32x2{bq.z, bq.w}, f); Gc[mm] = pk2(t.x, -t.y);
        t = cmul(w3, f); Gd[mm] = pk2(t.x, -t.y);
      }
      T[0] = mfma16(__builtin_bit_cast(bf16x8, F0), __builtin_bit_cast(bf16x8, Ga), T[0]);
      T[1] = mfma16(__builtin_bit_cast(bf16x8, F0), __builtin_bit_cast(bf16x8, Gb), T[1]);
      T[2] = mfma16(__builtin_bit_cast(bf16x8, F2), __builtin_bit_cast(bf16x8, Gc), T[2]);
      T[3] = mfma16(__builtin_bit_cast(bf16x8, F2), __builtin_bit_cast(bf16x8, Gd), T[3]);
    }
#pragma unroll
    for (int k = 0; k < 4; ++k) *(f32x4*)(smem + B_TAB + w * 4096 + (256 * k + 16 * c + 4 * g) * 4) = T[k];
  }
  ST(2);
  poll_flags(rs, FU_OFF + kap * 128, 16, tag, lane);
  u32x4 uc[2];
#pragma unroll
  for (int k = 0; k < 2; ++k)
    uc[k] = __builtin_amdgcn_raw_buffer_load_b128(rs, UT_OFF + (u32)d * 4096u + (u32)(n + 128 * k) * 16u, 0, AUX_SC1);
  LBAR();
  {
    const float* tab = (const float*)(smem + B_TAB + ch * 8192);
    float v[20];
#pragma unroll
    for (int k = 0; k < 19; ++k) {
      const int j = 2047 - 16 * n - k;
      const int lb = (int)rbf((float)(j < 0 ? 0 : j));
      const int ti = lb < 512 ? lb : 384 + (lb >> 2);
      const float t = tab[ti] + tab[1024 + ti];
      v[k] = (k >= 16 && j < 0) ? 0.f : t;
    }
    u32 E[9], O[9];
#pragma unroll
    for (int k = 0; k < 9; ++k) { E[k] = pk2(v[2 * k], v[2 * k + 1]); O[k] = pk2(v[2 * k + 1], v[2 * k + 2]); }
    char* rb = smem + B_R + ch * 4 * B_CS + n * 32;
    *(u32x4*)(rb) = u32x4{E[0], E[1], E[2], E[3]};
    *(u32x4*)(rb + 16) = u32x4{E[4], E[5], E[6], E[7]};
    *(u32x4*)(rb + B_CS) = u32x4{O[0], O[1], O[2], O[3]};
    *(u32x4*)(rb + B_CS + 16) = u32x4{O[4], O[5], O[6], O[7]};
    *(u32x4*)(rb + 2 * B_CS) = u32x4{E[1], E[2], E[3], E[4]};
    *(u32x4*)(rb + 2 * B_CS + 16) = u32x4{E[5], E[6], E[7], E[8]};
    *(u32x4*)(rb + 3 * B_CS) = u32x4{O[1], O[2], O[3], O[4]};
    *(u32x4*)(rb + 3 * B_CS + 16) = u32x4{O[5], O[6], O[7], O[8]};
  }
#pragma unroll
  for (int k = 0; k < 2; ++k) *(u32x4*)(smem + B_U + ch * 4608 + 512 + (n + 128 * k) * 16) = uc[k];
  LBAR();
  ST(3);
  f32x4 acc[8];
#pragma unroll
  for (int t = 0; t < 8; ++t) acc[t] = f32x4{0, 0, 0, 0};
  {
    const int beta = 15 - c;
    const u32 abase = (u32)(size_t)(LAS char*)(smem + B_R + ch * 4 * B_CS + (beta & 3) * B_CS + 16 * g + 2 * (beta & 12) + 2 * (2016 - 32 * half - 256 * 7 - 64 * 3));
    const char* ubase = smem + B_U + ch * 4608 + 512 + 2 * (-16 - 32 * half + 16 * c + 8 * g);
#pragma unroll
    for (int rr = 0; rr < 4; ++rr) {
      u32x2 alo[8], ahi[8];
      bf16x8 bfr[8];
#pragma unroll
      for (int mu = 0; mu < 8; ++mu) {
        asm volatile("ds_read_b64 %0, %2 offset:%3\n\tds_read_b64 %1, %2 offset:%4"
                     : "=&v"(alo[mu]), "=&v"(ahi[mu])
                     : "v"(abase), "i"(512 * (7 - mu) + 128 * (3 - rr)), "i"(512 * (7 - mu) + 128 * (3 - rr) + 8)
                     : "memory");
      }
#pragma unroll
      for (int tau = 0; tau < 8; ++tau) bfr[tau] = *(const bf16x8*)(ubase + 512 * tau - 128 * rr);
      WAIT_L0();
      __builtin_amdgcn_sched_barrier(0);
#pragma unroll
      for (int mu = 0; mu < 8; ++mu) {
        const bf16x8 a = __builtin_bit_cast(bf16x8, u32x4{alo[mu].x, alo[mu].y, ahi[mu].x, ahi[mu].y});
#pragma unroll
        for (int t = mu; t < 8; ++t) acc[t] = mfma16(a, bfr[t - mu], acc[t]);
      }
      __builtin_amdgcn_sched_barrier(0);
    }
  }
  LBAR();
  ST(4);
#pragma unroll
  for (int t = 0; t < 8; ++t) *(f32x4*)(smem + B_R + w * 8192 + (256 * t + 16 * c + 4 * g) * 4) = acc[t];
  LBAR();
#pragma unroll
  for (int k = 0; k < 2; ++k) {
    const char* pb = smem + B_R + ch * 16384 + (n + 128 * k) * 32;
    const f32x4 a0 = *(const f32x4*)(pb) + *(const f32x4*)(pb + 8192);
    const f32x4 a1 = *(const f32x4*)(pb + 16) + *(const f32x4*)(pb + 8192 + 16);
    u32x4 v = u32x4{pk2(a0.x, a0.y), pk2(a0.z, a0.w), pk2(a1.x, a1.y), pk2(a1.z, a1.w)};
    __builtin_amdgcn_raw_buffer_store_b128(v, rs, YT_OFF + (u32)d * 4096u + (u32)(n + 128 * k) * 16u, 0, AUX_SC1);
  }
  WAIT_V(0);
  LBAR();
  if (tid == 0) __builtin_amdgcn_raw_buffer_store_b32(tag, rs, FY_OFF + kap * 128 + mi * 4, 0, AUX_SC1);
  ST(5);
  const u32x2 dsk = *(const u32x2*)(pr.BD + 32 * kap + 4 * cq);
  poll_flags(rs, FY_OFF + kap * 128, 16, tag, lane);
  ST(6);
#pragma unroll
  for (int k = 0; k < 2; ++k) {
    const int row = 16 * k + (tid >> 4);
    u32x4 v = __builtin_amdgcn_raw_buffer_load_b128(rs, YT_OFF + (u32)(32 * kap + row) * 4096u + mi * 256u + c * 16u, 0, AUX_SC1);
    *(u32x4*)(smem + B_T + row * B_TS + c * 16) = v;
  }
  LBAR();
  {
    const char* tb = smem + B_T + (4 * cq) * B_TS + rp * 8;
    const u32x2 y0 = *(const u32x2*)(tb), y1 = *(const u32x2*)(tb + B_TS), y2 = *(const u32x2*)(tb + 2 * B_TS), y3 = *(const u32x2*)(tb + 3 * B_TS);
    const float d0 = bf2f(dsk.x), d1 = bfhi(dsk.x), d2 = bf2f(dsk.y), d3 = bfhi(dsk.y);
#pragma unroll
    for (int k = 0; k < 4; ++k) {
      const u32 a0 = (k & 2) ? y0.y : y0.x, a1 = (k & 2) ? y1.y : y1.x, a2 = (k & 2) ? y2.y : y2.x, a3 = (k & 2) ? y3.y : y3.x;
      const float c0 = (k & 1) ? bfhi(a0) : bf2f(a0), c1 = (k & 1) ? bfhi(a1) : bf2f(a1);
      const float c2 = (k & 1) ? bfhi(a2) : bf2f(a2), c3 = (k & 1) ? bfhi(a3) : bf2f(a3);
      const u32 s01 = pk2(bf2f(ut[k].x) * d0, bfhi(ut[k].x) * d1), s23 = pk2(bf2f(ut[k].y) * d2, bfhi(ut[k].y) * d3);
      const u32 r01 = pk2(c0 + bf2f(s01), c1 + bfhi(s01)), r23 = pk2(c2 + bf2f(s23), c3 + bfhi(s23));
      *(f32x4*)(pr.out + (size_t)(128 * mi + 4 * rp + k) * 1024 + 512 + 32 * kap + 4 * cq) =
          f32x4{bf2f(r01), bfhi(r01), bf2f(r23), bfhi(r23)};
    }
  }
  if (mi == 0 && tid == 0) __builtin_amdgcn_raw_buffer_store_b32(tag, rs, EPB_OFF + kap * 128, 0, AUX_SC1);
  ST(7);
}

__global__ __launch_bounds__(256, 2) void ssm_fused_kernel(Params pr) {
  extern __shared__ __attribute__((aligned(16))) char smem[];
  if (blockIdx.x < 256) partA(pr, smem);
  else partB(pr, smem);
}

extern "C" void kernel_launch(void* const* d_in, const int* in_sizes, int n_in, void* d_out, int out_size,
                              void* d_ws, size_t ws_size, hipStream_t stream) {
  Params p;
  p.x = (const u16*)d_in[0]; p.Lre = (const u16*)d_in[1]; p.Lim = (const u16*)d_in[2];
  p.ABre = (const u16*)d_in[3]; p.ABim = (const u16*)d_in[4]; p.ACre = (const u16*)d_in[5]; p.ACim = (const u16*)d_in[6];
  p.Alogdt = (const u16*)d_in[7]; p.AD = (const u16*)d_in[8];
  p.u = (const u16*)d_in[9]; p.Are = (const u16*)d_in[10]; p.Aim = (const u16*)d_in[11];
  p.BBre = (const u16*)d_in[12]; p.BBim = (const u16*)d_in[13]; p.BCre = (const u16*)d_in[14]; p.BCim = (const u16*)d_in[15];
  p.Blogdt = (const u16*)d_in[16]; p.BD = (const u16*)d_in[17];
  p.out = (float*)d_out; p.ws = (char*)d_ws;
  (void)hipFuncSetAttribute((const void*)ssm_fused_kernel, hipFuncAttributeMaxDynamicSharedMemorySize, LDS_BYTES);
  ssm_fused_kernel<<<512, 256, LDS_BYTES, stream>>>(p);
}
```

```cpp
#include <hip/hip_runtime.h>
#include <hip/hip_bf16.h>
#include <stdint.h>
#include <utility>

typedef unsigned short u16;
typedef unsigned u32;
typedef unsigned long long u64;
typedef short bf16x8 __attribute__((ext_vector_type(8)));
typedef short s16x4 __attribute__((ext_vector_type(4)));
typedef _Float16 f16x8 __attribute__((ext_vector_type(8)));
typedef float f32x4 __attribute__((ext_vector_type(4)));
typedef float f32x2 __attribute__((ext_vector_type(2)));
typedef float f32x16 __attribute__((ext_vector_type(16)));
typedef unsigned u32x4 __attribute__((ext_vector_type(4)));
typedef unsigned u32x2 __attribute__((ext_vector_type(2)));
typedef __amdgpu_buffer_rsrc_t rsrc_t;
#define GAS __attribute__((address_space(1)))
#define LAS __attribute__((address_space(3)))
#define CAS __attribute__((address_space(4)))
#define DEV __device__ __forceinline__

constexpr int N = 128;
constexpr u32 H_OFF = 0;
constexpr u32 UT_OFF = 4u << 20;
constexpr u32 YT_OFF = 6u << 20;
constexpr u32 HS_OFF = 8u << 20;
constexpr u32 US_OFF = HS_OFF + (512u << 10);
constexpr u32 YS_OFF = US_OFF + (256u << 10);
constexpr u32 EG_OFF = YS_OFF + (256u << 10);
constexpr u32 FC_OFF = EG_OFF + (128u << 10);
constexpr u32 EPA_OFF = FC_OFF + 65536;
constexpr u32 WS_USED = EPA_OFF + 128;
constexpr u32 DBG_OFF = 16u << 20;
constexpr int LDS_BYTES = 163840;
constexpr u32 SPIN_CAP = 1u << 20;
constexpr int AUX_SC1 = 16;
#define B7_AT 1
#define B10_AT 2
#define B11_Q 6
#define TOUCH 1
#define POLL_K 8
#define ASH 2
#define NPRE 8
#define YB_AT 16
#define OUT_A AUX_SC1
#define OUT_B AUX_SC1
#define ULATE 1
constexpr float LOG2E = 1.4426950408889634f, INV2PI = 0.15915494309189535f;

#define WAIT_V(n) asm volatile("s_waitcnt vmcnt(" #n ")" ::: "memory")
#define WAIT_L0() asm volatile("s_waitcnt lgkmcnt(0)" ::: "memory")
#define CWAIT_L0() do { __builtin_amdgcn_s_waitcnt(0xC07F); asm volatile("" ::: "memory"); } while (0)
#define CWAIT_L(n) do { __builtin_amdgcn_s_waitcnt(0xC07F | ((n) << 8)); asm volatile("" ::: "memory"); } while (0)
#define MEMFENCE() asm volatile("" ::: "memory")
#define BAR() do { __builtin_amdgcn_s_barrier(); asm volatile("" ::: "memory"); } while (0)
#define LBAR() do { CWAIT_L0(); BAR(); } while (0)
#define PRIO(n) do { __builtin_amdgcn_sched_barrier(0); __builtin_amdgcn_s_setprio(n); __builtin_amdgcn_sched_barrier(0); } while (0)
#define UNLIKELY(x) __builtin_expect(!!(x), 0)
#define SCHED() __builtin_amdgcn_sched_barrier(0)
#define PIN(x) asm volatile("" : "+v"(x))

#define STCOND(k) 1
#define ST(k) do {} while (0)

#define FST(k) do {} while (0)
DEV float bf2f(u32 v) { return __uint_as_float(v << 16); }
DEV float bfhi(u32 v) { return __uint_as_float(v & 0xffff0000u); }
DEV u32 pk2(float lo, float hi) {
  u32 r;
  asm("v_cvt_pk_bf16_f32 %0, %1, %2" : "=v"(r) : "v"(lo), "v"(hi));
  return r;
}
DEV int opq(int x) { asm volatile("" : "+v"(x)); return x; }
DEV float rbf(float x) { return (float)(__bf16)x; }
DEV f32x2 pfma(f32x2 a, f32x2 b, f32x2 c) { return __builtin_elementwise_fma(a, b, c); }
DEV f32x2 dup(float a) { f32x2 r = f32x2{a, a}; PIN(r); return r; }
DEV f32x2 lo2(f32x4 v) { return __builtin_shufflevector(v, v, 0, 1); }
DEV f32x2 hi2(f32x4 v) { return __builtin_shufflevector(v, v, 2, 3); }
struct c2 { f32x2 r, i; };
DEV c2 cmul2(c2 a, c2 b) { return c2{pfma(-a.i, b.i, a.r * b.r), pfma(a.i, b.r, a.r * b.i)}; }
DEV c2 cfma2(c2 a, c2 b, c2 c) { return c2{pfma(-a.i, b.i, pfma(a.r, b.r, c.r)), pfma(a.i, b.r, pfma(a.r, b.i, c.i))}; }
DEV c2 cpow4(c2 a) {
  f32x2 p = pfma(a.r, a.r, -(a.i * a.i)), q = a.r * a.i, v = q * 4.0f;
  return c2{pfma(-v, q, p * p), v * p};
}
DEV c2 cexp2(f32x2 zr, f32x2 zi, f32x2 k, bool reduce) {
  f32x2 a = zr * k, t = zi * k;
  if (reduce) t = f32x2{__builtin_amdgcn_fractf(t.x), __builtin_amdgcn_fractf(t.y)};
  f32x2 e = f32x2{__builtin_amdgcn_exp2f(a.x), __builtin_amdgcn_exp2f(a.y)};
  f32x2 cs = f32x2{__builtin_amdgcn_cosf(t.x), __builtin_amdgcn_cosf(t.y)};
  f32x2 sn = f32x2{__builtin_amdgcn_sinf(t.x), __builtin_amdgcn_sinf(t.y)};
  return c2{e * cs, e * sn};
}
DEV rsrc_t mkrs(const void* p, u32 bytes) { return __builtin_amdgcn_make_buffer_rsrc((void*)p, 0, bytes, 0x00020000); }
DEV void dma16(rsrc_t rs, char* l, u32 voff, u32 soff) {
  __builtin_amdgcn_raw_ptr_buffer_load_lds(rs, (LAS void*)l, 16, voff, soff, 0, 0);
}
DEV void st128(u32x4 v, rsrc_t rs, u32 off) { __builtin_amdgcn_raw_buffer_store_b128(v, rs, off, 0, AUX_SC1); }
DEV u32 next_tag(u32 e) { return e * 0x9E3779B1u + 0x7F4A7C15u; }
DEV f32x4 mfma16(bf16x8 a, bf16x8 b, f32x4 c) { return __builtin_amdgcn_mfma_f32_16x16x32_bf16(a, b, c, 0, 0, 0); }
DEV f32x16 mfma32(bf16x8 a, bf16x8 b, f32x16 c) { return __builtin_amdgcn_mfma_f32_32x32x16_bf16(a, b, c, 0, 0, 0); }
struct c1 { float r, i; };
DEV c1 cexp1(float zr, float zi, float k, bool reduce) {
  float t = zi * k;
  if (reduce) t = __builtin_amdgcn_fractf(t);
  const float e = __builtin_amdgcn_exp2f(zr * k);
  float a = e * __builtin_amdgcn_cosf(t), b = e * __builtin_amdgcn_sinf(t);
  PIN(a); PIN(b);
  return c1{a, b};
}
DEV c1 cfma1(c1 a, c1 b, c1 c) {
  float x = __builtin_fmaf(-a.i, b.i, __builtin_fmaf(a.r, b.r, c.r));
  PIN(x);
  float y = __builtin_fmaf(a.i, b.r, __builtin_fmaf(a.r, b.i, c.i));
  PIN(y);
  return c1{x, y};
}
DEV c1 cmul1(c1 a, c1 b) {
  float x = __builtin_fmaf(-a.i, b.i, a.r * b.r);
  PIN(x);
  float y = __builtin_fmaf(a.i, b.r, a.r * b.i);
  PIN(y);
  return c1{x, y};
}
DEV c1 csq1(c1 a) {
  float x = __builtin_fmaf(a.r, a.r, -(a.i * a.i));
  PIN(x);
  float y = (a.r + a.r) * a.i;
  PIN(y);
  return c1{x, y};
}
DEV u32 pkh(c1 a) { u32 r; asm("v_cvt_pk_f16_f32 %0, %1, %2" : "=v"(r) : "v"(a.r), "v"(a.i)); return r; }
template <bool CONJ> DEV f16x8 cmulh4(u32x4 a, u32x4 b) {
  u32 r0, r1, r2, r3;
#define MULH(D, A, B, M) "v_pk_mul_f16 " D ", " A ", " B " op_sel_hi:[0,1]" M "\n\t"
#define FMAH(D, A, B, M) "v_pk_fma_f16 " D ", " A ", " B ", " D " op_sel:[1,1,0] op_sel_hi:[1,0,1] neg_lo:[1,0,0]" M "\n\t"
  if (CONJ)
    asm(MULH("%0", "%4", "%8", " neg_hi:[1,0]") MULH("%1", "%5", "%9", " neg_hi:[1,0]") MULH("%2", "%6", "%10", " neg_hi:[1,0]") MULH("%3", "%7", "%11", " neg_hi:[1,0]")
        FMAH("%0", "%4", "%8", " neg_hi:[1,0,0]") FMAH("%1", "%5", "%9", " neg_hi:[1,0,0]") FMAH("%2", "%6", "%10", " neg_hi:[1,0,0]") FMAH("%3", "%7", "%11", " neg_hi:[1,0,0]")
        : "=&v"(r0), "=&v"(r1), "=&v"(r2), "=&v"(r3) : "v"(a.x), "v"(a.y), "v"(a.z), "v"(a.w), "v"(b.x), "v"(b.y), "v"(b.z), "v"(b.w));
  else
    asm(MULH("%0", "%4", "%8", "") MULH("%1", "%5", "%9", "") MULH("%2", "%6", "%10", "") MULH("%3", "%7", "%11", "")
        FMAH("%0", "%4", "%8", "") FMAH("%1", "%5", "%9", "") FMAH("%2", "%6", "%10", "") FMAH("%3", "%7", "%11", "")
        : "=&v"(r0), "=&v"(r1), "=&v"(r2), "=&v"(r3) : "v"(a.x), "v"(a.y), "v"(a.z), "v"(a.w), "v"(b.x), "v"(b.y), "v"(b.z), "v"(b.w));
#undef MULH
#undef FMAH
  return __builtin_bit_cast(f16x8, u32x4{r0, r1, r2, r3});
}
DEV void level_a(c1 e, c1& e2, u32& h1, u32& h2) {
  float t;
  asm("v_mul_f32 %4, %6, %6\n\tv_add_f32 %1, %5, %5\n\tv_cvt_pk_f16_f32 %2, %5, %6\n\t"
      "v_fma_f32 %0, %5, %5, -%4\n\tv_mul_f32 %1, %1, %6\n\tv_cvt_pk_f16_f32 %3, %0, %1"
      : "=&v"(e2.r), "=&v"(e2.i), "=&v"(h1), "=&v"(h2), "=&v"(t) : "v"(e.r), "v"(e.i));
}
template <bool NEXT> DEV void level_b(c1 e, c1 e2, u32& h3, c1& e4) {
  float x, y, t;
  if (NEXT)
    asm("v_mul_f32 %3, %8, %6\n\tv_mul_f32 %4, %8, %7\n\tv_mul_f32 %5, %9, %9\n\t"
        "v_fma_f32 %3, -%9, %7, %3\n\tv_fma_f32 %4, %9, %6, %4\n\tv_add_f32 %2, %8, %8\n\t"
        "v_fma_f32 %1, %8, %8, -%5\n\tv_cvt_pk_f16_f32 %0, %3, %4\n\tv_mul_f32 %2, %2, %9"
        : "=&v"(h3), "=&v"(e4.r), "=&v"(e4.i), "=&v"(x), "=&v"(y), "=&v"(t) : "v"(e.r), "v"(e.i), "v"(e2.r), "v"(e2.i));
  else
    asm("v_mul_f32 %1, %5, %3\n\tv_mul_f32 %2, %5, %4\n\tv_fma_f32 %1, -%6, %4, %1\n\tv_fma_f32 %2, %6, %3, %2\n\tv_cvt_pk_f16_f32 %0, %1, %2"
        : "=&v"(h3), "=&v"(x), "=&v"(y) : "v"(e.r), "v"(e.i), "v"(e2.r), "v"(e2.i));
}
DEV u32x4 rs4of(const void* p, u32 bytes) { return u32x4{(u32)(u64)p, (u32)((u64)p >> 32) & 0xffffu, bytes, 0x00020000u}; }
#define SETM0(x) asm volatile("s_mov_b32 m0, %0\n\ts_nop 0" :: "s"(x) : "memory")
#define DMA(voff, rs, soff, IMM) asm volatile("buffer_load_dwordx4 %0, %1, %2 offen offset:%3 lds" :: "v"(voff), "s"(rs), "s"(soff), "i"(IMM) : "memory")
DEV void wstep(c1& W, c1& V, c1 e, c1 f, u32& hw, u32& hv) {
  float a, b;
  asm("v_cvt_pk_f16_f32 %4, %0, %1\n\tv_cvt_pk_f16_f32 %5, %2, %3\n\t"
      "v_mul_f32 %6, %0, %9\n\tv_mul_f32 %7, %2, %11\n\tv_mul_f32 %0, %0, %8\n\tv_mul_f32 %2, %2, %10\n\t"
      "v_fma_f32 %0, -%1, %9, %0\n\tv_fma_f32 %2, -%3, %11, %2\n\tv_fma_f32 %1, %1, %8, %6\n\tv_fma_f32 %3, %3, %10, %7"
      : "+v"(W.r), "+v"(W.i), "+v"(V.r), "+v"(V.i), "=&v"(hw), "=&v"(hv), "=&v"(a), "=&v"(b)
      : "v"(e.r), "v"(e.i), "v"(f.r), "v"(f.i));
}
DEV f16x8 ash(u32x4 v) { return __builtin_bit_cast(f16x8, v); }
DEV f32x4 mfma16h(f16x8 a, f16x8 b, f32x4 c) { return __builtin_amdgcn_mfma_f32_16x16x32_f16(a, b, c, 0, 0, 0); }
DEV u32 wave_max(u32 v) {
  v = max(v, (u32)__builtin_amdgcn_update_dpp(0, (int)v, 0xB1, 0xf, 0xf, true));
  v = max(v, (u32)__builtin_amdgcn_update_dpp(0, (int)v, 0x4E, 0xf, 0xf, true));
  v = max(v, (u32)__builtin_amdgcn_update_dpp(0, (int)v, 0x141, 0xf, 0xf, true));
  v = max(v, (u32)__builtin_amdgcn_update_dpp(0, (int)v, 0x140, 0xf, 0xf, true));
  auto a = __builtin_amdgcn_permlane16_swap(v, v, false, false);
  v = max(a[0], a[1]);
  auto b = __builtin_amdgcn_permlane32_swap(v, v, false, false);
  return max(b[0], b[1]);
}
template <int K> DEV f32x2 pr16(f32x16 v) { return __builtin_shufflevector(v, v, 2 * K, 2 * K + 1); }
DEV u32 sum4(u32 m, u32x4 v) { return (m + v.x + v.y) + (v.z + v.w); }
DEV bf16x8 asb(u32x4 v) { return __builtin_bit_cast(bf16x8, v); }
DEV float swapadd32(float a, float b) {
  auto r = __builtin_amdgcn_permlane32_swap(__float_as_uint(a), __float_as_uint(b), false, false);
  return __uint_as_float(r[0]) + __uint_as_float(r[1]);
}
DEV float swapadd16(float a, float b) {
  auto r = __builtin_amdgcn_permlane16_swap(__float_as_uint(a), __float_as_uint(b), false, false);
  return __uint_as_float(r[0]) + __uint_as_float(r[1]);
}

template <int V> struct IC { static constexpr int value = V; };
template <int... I, class F> DEV void sfor_impl(std::integer_sequence<int, I...>, F&& f) { (f(IC<I>{}), ...); }
template <int N, class F> DEV void sfor(F&& f) { sfor_impl(std::make_integer_sequence<int, N>{}, f); }
#define INL __attribute__((always_inline))
struct Params {
  const u16 *x, *Lre, *Lim, *ABre, *ABim, *ACre, *ACim, *Alogdt, *AD;
  const u16 *u, *Are, *Aim, *BBre, *BBim, *BCre, *BCim, *Blogdt, *BD;
  float* out;
  char* ws;
};

constexpr int L_XR = 0;
constexpr int L_BR = 49152;
constexpr int B_CS = 4160;
constexpr int L_R = 0;
constexpr int L_U = 33280;
constexpr int L_CT = 42496;
constexpr int L_CI = 75264;
constexpr int L_WT = 140800;
constexpr int L_SM = 141824;
constexpr int L_CNT = 144128;
constexpr int L_AP = 144384;
constexpr int L_PF = 145408;
constexpr int L_UT = 83456;
constexpr int L_BP = 153856;
constexpr int L_TAB = 145664;
constexpr int L_YT = 0;
constexpr int B_TW = 4352, B_TS = 272;

#define CODE_LINES 178
#define FDUMP(N) do {} while (0)

DEV int cbase(int w, int im, int jt) { return (im != (jt >> 1)) ? L_CI + w * 16384 + jt * 4096 : L_CT + w * 8192 + (jt & 1) * 4096; }
struct CStage {
  u32x4 rsCr, rsCi;
  u32 vC[4], sC, lds0;
  int w;
  DEV void init(const Params& pr, u32 l0, int w_, int ct, int lane) {
    rsCr = rs4of(pr.ACre, 512u << 10); rsCi = rs4of(pr.ACim, 512u << 10);
    sC = (u32)(64 * ct) * 1024u + w_ * 256; lds0 = l0; w = w_;
#pragma unroll
    for (int q = 0; q < 4; ++q) { vC[q] = (((u32)(lane >> 4) * 4096u + (u32)((lane & 15) ^ (lane >> 4)) * 16u) ^ (64u * q)) + q * 15360u; PIN(vC[q]); }
  }
  DEV void go(int im, int i) {
    if ((i & 3) == 0) SETM0(lds0 + cbase(w, im, i >> 2));
    DMA(vC[i & 3], im ? rsCi : rsCr, sC + (i >> 2) * 1024, (i & 3) * 1024);
  }
};

#define PINARGS() asm volatile("" :: "s"(pr.x), "s"(pr.Lre), "s"(pr.Lim), "s"(pr.ABre), "s"(pr.ABim), "s"(pr.ACre), "s"(pr.ACim), "s"(pr.Alogdt), "s"(pr.AD), "s"(pr.u), "s"(pr.Are), "s"(pr.Aim), "s"(pr.BBre), "s"(pr.BBim), "s"(pr.BCre), "s"(pr.BCim), "s"(pr.Blogdt), "s"(pr.BD), "s"(pr.out), "s"(pr.ws))
DEV void partA(const Params& pr, char* smem, const u32 lds0) {
  char* ws = pr.ws;
  const int b = blockIdx.x;
  const int r = 2 * (b & 7) + (b >> 7), s = (b >> 3) & 15;
  const int tid = threadIdx.x, lane = tid & 63;
  const int w = __builtin_amdgcn_readfirstlane(tid >> 6);
  const int c = lane & 15, g = lane >> 4, p = lane & 31, hh = lane >> 5;
  ST(0);
  FST(0);
  const int lr = lane >> 3;
  const u32 sw = ((lane & 7) ^ (lr >> 1)) * 16;
  PINARGS();
  const u32x4 rsX = rs4of(pr.x, 2u << 20), rsB = rs4of((w < 2) ? pr.ABre : pr.ABim, 512u << 10);
  u32 vX[4], vB[2];
  vX[0] = (u32)(16 * (lr >> 2) + 8 * (lr & 1) + ((lr >> 1) & 1)) * 1024u + sw;
  vX[1] = (vX[0] ^ 64u) + 1024u; vX[2] = vX[0] + 2048u; vX[3] = vX[1] + 2048u;
  vB[0] = (u32)lr * 1024u + sw; vB[1] = (vB[0] ^ 64u) + 7168u;
  const u32 sX = (u32)(128 * r + 32 * w) * 1024u, sB = (u32)(32 * s + 16 * (w & 1)) * 1024u;
  const u32 rot = (u32)(s + r) * 128u;
  const u32 mX = lds0 + L_XR + w * 4096, mB = lds0 + L_BR + w * 2048;
  auto kc = [&](int it) { return (rot + it * 128u) & 0x380u; };
  auto dma_x = [&](int it, int q) {
    if (q == 0) SETM0(mX + (it % 3) * 16384);
    DMA(vX[q], rsX, sX + kc(it), q * 1024);
  };
  auto dma_b = [&](int it, int q) {
    if (q == 0) SETM0(mB + (it % 3) * 8192);
    DMA(vB[q], rsB, sB + kc(it), q * 1024);
  };
#pragma unroll
  for (int q = 0; q < 4; ++q) dma_x(0, q);
  dma_b(0, 0); dma_b(0, 1);
  SCHED();
  u32 tag = *(const CAS u32*)(ws + EPA_OFF);
#pragma unroll
  for (int q = 0; q < 4; ++q) dma_x(1, q);
  dma_b(1, 0); dma_b(1, 1);
  PRIO(2);
  const rsrc_t rs = mkrs(ws, WS_USED);
  int fb[4], fx[4];
#pragma unroll
  for (int k = 0; k < 4; ++k) {
    fb[k] = L_BR + ((p * 128 + ((hh ^ ((p >> 1) & 7)) * 16)) ^ (32 * k));
    fx[k] = fb[k] + (w * 4096 - L_BR);
    PIN(fb[k]); PIN(fx[k]);
    SCHED();
    dma_x(2, k);
  }
  SCHED();
  FST(1);

  float zr, zi, lre, lim, av, q16, nrA;
  c1 lam, coef, L8, L16, om, pwA, wH0, wH1;
  c2 lamd;
  u32 praw[3];
  const int rh = s >> 3, ct = s & 7;
  auto micro = [&](auto ID) INL {
    constexpr int id = decltype(ID)::value;
    if (id == 8) { const u16* pp = (const u16*)(smem + L_AP + w * 256); praw[0] = pp[p]; praw[1] = pp[32 + p]; praw[2] = pp[64 + p]; }
    if (id == 14) {
      lre = bf2f(praw[0]); lim = bf2f(praw[1]);
      av = rbf(__builtin_amdgcn_exp2f(bf2f(praw[2]) * LOG2E));
      PIN(av); PIN(lre); PIN(lim);
    }
    if (id == 18) {
      zi = av * lim * INV2PI; zi -= __builtin_rintf(zi);
      av *= lre; zr = av * LOG2E;
      q16 = (float)(32 * w + 16 * (opq(lane) >> 5));
      PIN(zr); PIN(zi); PIN(av); PIN(q16);
      asm volatile("" : "+s"(tag)); tag = next_tag(tag); asm volatile("" : "+s"(tag));
    }
    if (id == 22) lam = cexp1(zr, zi, 1.f, false);
    if (id == 26) L8 = cexp1(zr, zi, 8.f, false);
    if (id == 30) om = cexp1(zr, zi, 112.f - q16, false);
    if (id == 34) {
      float sm = __builtin_fmaf(av, 1.f / 120.f, 1.f / 24.f);
      sm = __builtin_fmaf(sm, av, 1.f / 6.f); sm = __builtin_fmaf(sm, av, 0.5f); sm = __builtin_fmaf(sm, av, 1.f) * av;
      float lg = __builtin_amdgcn_exp2f(zr) - 1.f;
      PIN(sm); PIN(lg);
      av = (fabsf(av) < 0.125f) ? sm : lg;
      PIN(av);
    }
    if (id == 38) { const float sh = __builtin_amdgcn_sinf(0.5f * zi); nrA = av * __builtin_amdgcn_cosf(zi) - 2.0f * sh * sh; PIN(nrA); }
    if (id == 43) { av = 2.0f * __builtin_amdgcn_rcpf(lre * lre + lim * lim); PIN(av); lamd.r = dup(lam.r); }
    if (id == 45) { coef.r = (nrA * lre + lam.i * lim) * av; PIN(coef.r); coef.i = (lam.i * lre - nrA * lim) * av; PIN(coef.i); }
    if (id == 47) { lamd.i = dup(lam.i); }
    SCHED();
  };

  f32x16 accR, accI;
#pragma unroll
  for (int i = 0; i < 16; ++i) { accR[i] = 0.f; accI[i] = 0.f; }
  bf16x8 af[2][4], bf[2][8];
  sfor<9>([&](auto IT) INL {
    constexpr int it = decltype(IT)::value, cu = it & 1, pv = cu ^ 1;
    auto rdx = [&](int k) { if (it < 8) af[cu][k] = *(const bf16x8*)(smem + (it % 3) * 16384 + fx[k]); };
    auto rdb = [&](int k) { if (it < 8) bf[cu][k] = *(const bf16x8*)(smem + (it % 3) * 8192 + (k >> 2) * 4096 + fb[k & 3]); };
    CWAIT_L0();
    FST(2 + 2 * it);
    if (it < 7) {
      if (it == 0) WAIT_V(10); else if (it < 6) WAIT_V(10); else WAIT_V(0);
      BAR();
    }
    FST(3 + 2 * it);
    SCHED();
    sfor<8>([&](auto J) INL {
      constexpr int j = decltype(J)::value;
      if (it > 0) {
        if (j & 1) accI = mfma32(af[pv][j >> 1], bf[pv][4 + (j >> 1)], accI);
        else accR = mfma32(af[pv][j >> 1], bf[pv][j >> 1], accR);
      }
      SCHED();
      if constexpr (j < 2) { rdx(2 * j); rdx(2 * j + 1); } else if constexpr (j < 6) { rdb(j - 2); rdb(j + 2); }
      SCHED();
      if constexpr (it < 6 && j < 2) dma_b(it + 2, j);
      if constexpr (it < 5 && j >= 3 && j < 7) {
        if (j == 3) CWAIT_L(4);
        dma_x(it + 3, j - 3);
      }
      SCHED();
      micro(IC<8 * it + j>{});
    });
  });
  FST(20);
  ST(1);

  c2 z2[8] = {c2{pr16<0>(accR), pr16<0>(accI)}, c2{pr16<1>(accR), pr16<1>(accI)}, c2{pr16<2>(accR), pr16<2>(accI)}, c2{pr16<3>(accR), pr16<3>(accI)},
              c2{pr16<4>(accR), pr16<4>(accI)}, c2{pr16<5>(accR), pr16<5>(accI)}, c2{pr16<6>(accR), pr16<6>(accI)}, c2{pr16<7>(accR), pr16<7>(accI)}};
  c2 en;
  {
    f32x2 ar, ai;
#define STEP(DR, DI, SR, SI, ZR, ZI) \
    "v_pk_fma_f32 " DR ", %4, " SR ", " ZR "\n\tv_pk_fma_f32 " DI ", %4, " SI ", " ZI "\n\t" \
    "v_pk_fma_f32 " DR ", %5, " SI ", " DR " neg_lo:[1,0,0] neg_hi:[1,0,0]\n\tv_pk_fma_f32 " DI ", %5, " SR ", " DI "\n\t"
    asm("s_nop 11\n\t"
        STEP("%0", "%1", "%6", "%7", "%8", "%9") STEP("%2", "%3", "%0", "%1", "%10", "%11") STEP("%0", "%1", "%2", "%3", "%12", "%13")
        STEP("%2", "%3", "%0", "%1", "%14", "%15") STEP("%0", "%1", "%2", "%3", "%16", "%17") STEP("%2", "%3", "%0", "%1", "%18", "%19")
        STEP("%0", "%1", "%2", "%3", "%20", "%21")
        : "=&v"(en.r), "=&v"(en.i), "=&v"(ar), "=&v"(ai)
        : "v"(lamd.r), "v"(lamd.i), "v"(z2[0].r), "v"(z2[0].i), "v"(z2[1].r), "v"(z2[1].i), "v"(z2[2].r), "v"(z2[2].i), "v"(z2[3].r), "v"(z2[3].i),
          "v"(z2[4].r), "v"(z2[4].i), "v"(z2[5].r), "v"(z2[5].i), "v"(z2[6].r), "v"(z2[6].i), "v"(z2[7].r), "v"(z2[7].i));
#undef STEP
  }
  const c1 eA = c1{en.r.x, en.i.x};
  {
    const c1 S = cfma1(L8, eA, c1{en.r.y, en.i.y});
    const c1 t = cmul1(om, S);
    *(float*)(smem + L_WT + w * 256 + p * 8 + hh * 4) = swapadd32(t.r, t.i);
    *(f32x2*)(smem + L_SM + (1 + 2 * w + hh) * 256 + p * 8) = f32x2{S.r, S.i};
  }
  LBAR();
  if (w == 0) {
    const f32x2* Wt = (const f32x2*)(smem + L_WT) + p;
    const f32x2 e = (Wt[0] + Wt[32]) + (Wt[64] + Wt[96]);
    st128(u32x4{__float_as_uint(e.x), tag, __float_as_uint(e.y), tag}, rs, EG_OFF + (u32)(s * 16 + r) * 512u + (u32)p * 16u);
  }
  PRIO(ASH);
  FST(21);
  ST(2);
  CStage cs;
  cs.init(pr, lds0, w, ct, lane);
  bf16x8 cre[4][4], cim[4][4];
  int cfo[4];
#pragma unroll
  for (int q = 0; q < 4; ++q) { cfo[q] = c * 256 + (((4 * q + g) ^ c) * 16); PIN(cfo[q]); }
  const c2 cd = c2{dup(coef.r), dup(coef.i)};
  f32x2 sm[7];
#pragma unroll
  for (int k = 0; k < 7; ++k) sm[k] = *(const f32x2*)(smem + L_SM + max(2 * w + hh - 6 + k, 0) * 256 + p * 8);
  L16 = cmul1(L8, L8);
  const u32x4 rs4 = rs4of(ws, WS_USED);
  const int r0 = 4 * w + hh, rm = max(r - 1, 0);
  const u32 o0 = EG_OFF + (u32)((s * 16 + min(r0, rm)) * 32 + p) * 16u, o1 = EG_OFF + (u32)((s * 16 + min(r0 + 2, rm)) * 32 + p) * 16u;
  u32x4 a0, a1;
  auto poll = [&]() {
    asm volatile("buffer_load_dwordx4 %0, %2, %4, 0 offen sc1\n\tbuffer_load_dwordx4 %1, %3, %4, 0 offen sc1"
                 : "=&v"(a0), "=&v"(a1) : "v"(o0), "v"(o1), "s"(rs4) : "memory");
  };
  SCHED();
#pragma unroll
  for (int k = 0; k < 8; ++k) {
    if (k == POLL_K) poll();
    if (k < NPRE) cs.go(0, k);
    const c2 a = z2[k];
    z2[k] = c2{pfma(-cd.i, a.i, cd.r * a.r), pfma(-cd.i, a.r, -(cd.r * a.i))};
    PIN(z2[k].r); PIN(z2[k].i);
    SCHED();
  }
  if (POLL_K == 8) poll();
  FST(22);
  c1 lcA, lcB, pwB;
  c2 lcp, pwp;
  u32 two0, two1, tro0, tro1, hso, hso1;
  {
    c1 lc = c1{sm[0].x, sm[0].y};
#pragma unroll
    for (int k = 1; k < 7; ++k) lc = cfma1(L16, lc, c1{sm[k].x, sm[k].y});
    pwA = cmul1(coef, cexp1(zr, zi, q16, false));
    lcA = cmul1(coef, lc); lcB = cmul1(coef, cfma1(L8, lc, eA)); pwB = cmul1(pwA, L8);
    lcp = c2{f32x2{lcA.r, lcB.r}, f32x2{lcA.i, lcB.i}}; pwp = c2{f32x2{pwA.r, pwB.r}, f32x2{pwA.i, pwB.i}};
    PIN(lcp.r); PIN(lcp.i); PIN(pwp.r); PIN(pwp.i);
    { wH0 = cexp1(zr, zi, (float)(128 * max(r - 1 - r0, 0)), true); const float m = r0 < r ? 1.f : 0.f; wH0.r *= m; wH0.i *= m; PIN(wH0.r); PIN(wH0.i); }
    { wH1 = cexp1(zr, zi, (float)(128 * max(r - 3 - r0, 0)), true); const float m = r0 + 2 < r ? 1.f : 0.f; wH1.r *= m; wH1.i *= m; PIN(wH1.r); PIN(wH1.i); }
    const int wsw = ((p >> 1) & 3) ^ (((p >> 3) & 1) << 1), f0 = (c >> 3) ^ ((g & 1) << 1), cb = (c >> 1) & 1;
    const u32 trb = lds0 + L_CT + w * 8192;
    two0 = trb + p * 64 + (((2 * hh) ^ wsw) * 16); two1 = two0 ^ 16;
    tro0 = trb + (8 * g + (c >> 2)) * 64 + (c & 1) * 8 + ((cb ^ f0) * 16); tro1 = tro0 ^ 32;
    hso = H_OFF + (u32)(((8 * r + 2 * w) * 16 + s) * 2) * 1024u + lane * 16u; hso1 = hso + 32768u;
    PIN(two0); PIN(two1); PIN(tro0); PIN(tro1); PIN(hso); PIN(hso1);
  }
  SCHED();
  FST(23);
  WAIT_V(0);
#pragma unroll
  for (int jt = 0; jt < 2; ++jt)
#pragma unroll
    for (int q = 0; q < 4; ++q) cre[q][jt] = *(const bf16x8*)(smem + cbase(w, 0, jt) + cfo[q]);
  SCHED();
  FST(24);
  PRIO(3);
  c1 H;
  {
    asm volatile("s_waitcnt vmcnt(0)" : "+v"(a0), "+v"(a1) :: "memory");
    auto ok = [&]() { return __all(a0.y == tag && a0.w == tag && a1.y == tag && a1.w == tag); };
    if (UNLIKELY(!ok())) {
#pragma unroll 1
      for (u32 spins = 0; spins < SPIN_CAP; ++spins) {
        poll();
        asm volatile("s_waitcnt vmcnt(0)" : "+v"(a0), "+v"(a1) :: "memory");
        if (ok()) break;
        __builtin_amdgcn_s_sleep(2);
      }
    }
    const c1 t = cfma1(wH1, c1{__uint_as_float(a1.x), __uint_as_float(a1.z)}, cmul1(wH0, c1{__uint_as_float(a0.x), __uint_as_float(a0.z)}));
    *(float*)(smem + L_AP + w * 256 + p * 8 + hh * 4) = swapadd32(t.r, t.i);
    FST(25);
    LBAR();
    const f32x2* Hp = (const f32x2*)(smem + L_AP) + p;
    const f32x2 h0 = Hp[0], h1 = Hp[32], h2 = Hp[64], h3 = Hp[96];
    H.r = (h0.x + h1.x) + (h2.x + h3.x); PIN(H.r);
    H.i = (h0.y + h1.y) + (h2.y + h3.y); PIN(H.i);
  }
  FST(26);
  ST(3);
  {
    const f32x2 hr = dup(H.r), hi = dup(H.i);
    f32x2 sr = pfma(pwp.r, hr, lcp.r), si = pfma(-pwp.r, hi, -lcp.i);
    sr = pfma(-pwp.i, hi, sr); si = pfma(-pwp.i, hr, si);
#define STEP(SR, SI, ZR, ZI) \
    "v_pk_fma_f32 " ZR ", %8, " SR ", " ZR "\n\tv_pk_fma_f32 " ZI ", %8, " SI ", " ZI "\n\t" \
    "v_pk_fma_f32 " ZR ", %9, " SI ", " ZR "\n\tv_pk_fma_f32 " ZI ", %9, " SR ", " ZI " neg_lo:[1,0,0] neg_hi:[1,0,0]\n\t"
    asm("s_nop 0\n\t" STEP("%10", "%11", "%0", "%1") STEP("%0", "%1", "%2", "%3") STEP("%2", "%3", "%4", "%5") STEP("%4", "%5", "%6", "%7")
        : "+v"(z2[0].r), "+v"(z2[0].i), "+v"(z2[1].r), "+v"(z2[1].i), "+v"(z2[2].r), "+v"(z2[2].i), "+v"(z2[3].r), "+v"(z2[3].i)
        : "v"(lamd.r), "v"(lamd.i), "v"(sr), "v"(si));
    asm(STEP("%10", "%11", "%0", "%1") STEP("%0", "%1", "%2", "%3") STEP("%2", "%3", "%4", "%5") STEP("%4", "%5", "%6", "%7")
        : "+v"(z2[4].r), "+v"(z2[4].i), "+v"(z2[5].r), "+v"(z2[5].i), "+v"(z2[6].r), "+v"(z2[6].i), "+v"(z2[7].r), "+v"(z2[7].i)
        : "v"(lamd.r), "v"(lamd.i), "v"(z2[3].r), "v"(z2[3].i));
#undef STEP
    u32x2 fl[2][2], fh[2][2];
#pragma unroll
    for (int pp = 0; pp < 2; ++pp) {
      auto cv = [&](int k) { return pp ? z2[k].i : z2[k].r; };
      *(LAS u32x4*)(size_t)two0 = u32x4{pk2(cv(0).x, cv(1).x), pk2(cv(2).x, cv(3).x), pk2(cv(4).x, cv(5).x), pk2(cv(6).x, cv(7).x)};
      *(LAS u32x4*)(size_t)two1 = u32x4{pk2(cv(0).y, cv(1).y), pk2(cv(2).y, cv(3).y), pk2(cv(4).y, cv(5).y), pk2(cv(6).y, cv(7).y)};
      MEMFENCE();
#pragma unroll
      for (int tt = 0; tt < 2; ++tt)
        asm volatile("ds_read_b64_tr_b16 %0, %2\n\tds_read_b64_tr_b16 %1, %3 offset:256"
                     : "=&v"(fl[pp][tt]), "=&v"(fh[pp][tt]) : "v"(tt ? tro1 : tro0), "v"(tt ? tro0 : tro1) : "memory");
    }
    asm volatile("s_waitcnt lgkmcnt(0)" : "+v"(fl[0][0]), "+v"(fh[0][0]), "+v"(fl[0][1]), "+v"(fh[0][1]), "+v"(fl[1][0]), "+v"(fh[1][0]), "+v"(fl[1][1]), "+v"(fh[1][1]) :: "memory");
    if (w == 3) __builtin_amdgcn_raw_buffer_store_b32(tag, rs, FC_OFF + (u32)(r * 16 + s) * 128u + p * 4u, 0, AUX_SC1);
    u32x4 fr[2][2];
#pragma unroll
    for (int pp = 0; pp < 2; ++pp)
#pragma unroll
      for (int tt = 0; tt < 2; ++tt) {
        fr[pp][tt] = u32x4{fl[pp][tt].x, fl[pp][tt].y, fh[pp][tt].x, fh[pp][tt].y};
        st128(fr[pp][tt], rs, (tt ? hso1 : hso) + pp * 1024u);
      }
    SCHED();
    __builtin_amdgcn_raw_buffer_store_b64(u32x2{sum4(sum4(tag, fr[0][0]), fr[1][0]), sum4(sum4(tag, fr[0][1]), fr[1][1])}, rs,
                                          HS_OFF + (u32)((r * 4 + w) * 16 + s) * 512u + lane * 8u, 0, AUX_SC1);
  }
  SCHED();
  FST(27);
  ST(4);
  BAR();
  PRIO(2);
  const u32 of = FC_OFF + (u32)(r * 16 + (lane & 15)) * 128u;
  const int row0 = 128 * r + 64 * rh + 16 * w + 4 * g, col0 = 64 * ct + 4 * c;
  u32 fl;
  u32x2 xs[4], dk;
#pragma unroll
  for (int i = 8; i < 16; ++i) cs.go(0, i);
#pragma unroll
  for (int i = 0; i < 8; ++i) cs.go(1, i);
  {
    const u32x4 rd4 = rs4of(pr.AD, 1024u);
    asm volatile("buffer_load_dword %0, %6, %7, 0 offen sc1\n\t"
                 "buffer_load_dwordx2 %1, %8, %9, 0 offen\n\tbuffer_load_dwordx2 %2, %8, %9, 0 offen offset:1024\n\t"
                 "buffer_load_dwordx2 %3, %8, %9, 0 offen offset:2048\n\tbuffer_load_dwordx2 %4, %8, %9, 0 offen offset:3072\n\t"
                 "buffer_load_dwordx2 %5, %10, %11, 0 offen"
                 : "=&v"(fl), "=&v"(xs[0]), "=&v"(xs[1]), "=&v"(xs[2]), "=&v"(xs[3]), "=&v"(dk)
                 : "v"(of), "s"(rs4), "v"((u32)row0 * 1024u + (u32)col0 * 2u), "s"(rsX), "v"((u32)col0 * 2u), "s"(rd4) : "memory");
  }
#pragma unroll
  for (int i = 8; i < 16; ++i) cs.go(1, i);
  asm volatile("s_waitcnt vmcnt(8)" : "+v"(fl), "+v"(xs[0]), "+v"(xs[1]), "+v"(xs[2]), "+v"(xs[3]), "+v"(dk) :: "memory");
  if (UNLIKELY(!__all(fl == tag))) {
#pragma unroll 1
    for (u32 spins = 0; spins < SPIN_CAP; ++spins) {
      asm volatile("buffer_load_dword %0, %1, %2, 0 offen sc1\n\ts_waitcnt vmcnt(0)" : "=&v"(fl) : "v"(of), "s"(rs4) : "memory");
      if (__all(fl == tag)) break;
      __builtin_amdgcn_s_sleep(6);
    }
  }
  FST(28);

  const u32 hb = H_OFF + (u32)(8 * r + 4 * rh) * 32768u + (u32)w * 8192u;
  const u32 l16 = lane * 16u;
  u32x4 hf[2][8];
  auto hoff = [&](int k) { return hb + (u32)((w + 1 + k) & 3) * 32768u; };
#define LOADH(dst, SO, IMM) asm volatile("buffer_load_dwordx4 %0, %1, %2, %3 offen offset:" #IMM " sc1" : "=v"(dst) : "v"(l16), "s"(rs4), "s"(SO) : "memory")
  auto loadq = [&](u32x4& dst, u32 base, int q) {
    const u32 so = base + (q >> 2) * 4096u;
    if ((q & 3) == 0) LOADH(dst, so, 0); else if ((q & 3) == 1) LOADH(dst, so, 1024); else if ((q & 3) == 2) LOADH(dst, so, 2048); else LOADH(dst, so, 3072);
  };
  {
    const float dd[4] = {bf2f(dk.x), bfhi(dk.x), bf2f(dk.y), bfhi(dk.y)};
#pragma unroll
    for (int i = 0; i < 16; ++i) {
      loadq(hf[i >> 3][i & 7], hoff(i >> 3), i & 7);
      if (i < 4) { cre[i][2] = *(const bf16x8*)(smem + cbase(w, 0, 2) + cfo[i]); cre[i][3] = *(const bf16x8*)(smem + cbase(w, 0, 3) + cfo[i]); }
      else if (i < 8) { cim[i & 3][0] = *(const bf16x8*)(smem + cbase(w, 1, 0) + cfo[i & 3]); cim[i & 3][1] = *(const bf16x8*)(smem + cbase(w, 1, 1) + cfo[i & 3]); }
      else if (i < 12) {
        if (i == 8) WAIT_V(9);
        cim[i & 3][2] = *(const bf16x8*)(smem + cbase(w, 1, 2) + cfo[i & 3]); cim[i & 3][3] = *(const bf16x8*)(smem + cbase(w, 1, 3) + cfo[i & 3]);
      } else {
        const int k = i & 3;
        const u32 s01 = pk2(bf2f(xs[k].x) * dd[0], bfhi(xs[k].x) * dd[1]), s23 = pk2(bf2f(xs[k].y) * dd[2], bfhi(xs[k].y) * dd[3]);
        xs[k] = u32x2{s01, s23};
        PIN(xs[k]);
      }
      if (i == B10_AT) BAR();
      SCHED();
    }
  }
  CWAIT_L0();
  FST(29);
  f32x4 y[2][4], y0[4], pt[3][4];
  auto skipinit = [&](int jt) {
    return f32x4{(jt & 1) ? bfhi(xs[0][jt >> 1]) : bf2f(xs[0][jt >> 1]), (jt & 1) ? bfhi(xs[1][jt >> 1]) : bf2f(xs[1][jt >> 1]),
                 (jt & 1) ? bfhi(xs[2][jt >> 1]) : bf2f(xs[2][jt >> 1]), (jt & 1) ? bfhi(xs[3][jt >> 1]) : bf2f(xs[3][jt >> 1])};
  };
  auto park = [&](int k, int jt, f32x4 v) { *(f32x4*)(smem + L_CI + w * 16384 + k * 4096 + jt * 1024 + lane * 16) = v; };
  const u32 sgo = HS_OFF + (u32)((r * 4 + 2 * rh) * 16 + 4 * w) * 512u;
  const u32 l8 = lane * 8u;
  u32x2 sg[8];
  auto loadsg = [&]() {
    asm volatile("buffer_load_dwordx2 %0, %8, %9, %10 offen sc1\n\tbuffer_load_dwordx2 %1, %8, %9, %10 offen offset:512 sc1\n\t"
                 "buffer_load_dwordx2 %2, %8, %9, %10 offen offset:1024 sc1\n\tbuffer_load_dwordx2 %3, %8, %9, %10 offen offset:1536 sc1\n\t"
                 "buffer_load_dwordx2 %4, %8, %9, %11 offen sc1\n\tbuffer_load_dwordx2 %5, %8, %9, %11 offen offset:512 sc1\n\t"
                 "buffer_load_dwordx2 %6, %8, %9, %11 offen offset:1024 sc1\n\tbuffer_load_dwordx2 %7, %8, %9, %11 offen offset:1536 sc1"
                 : "=&v"(sg[0]), "=&v"(sg[1]), "=&v"(sg[2]), "=&v"(sg[3]), "=&v"(sg[4]), "=&v"(sg[5]), "=&v"(sg[6]), "=&v"(sg[7])
                 : "v"(l8), "s"(rs4), "s"(sgo), "s"(sgo + 8192u) : "memory");
  };
#define WAITSG(N) asm volatile("s_waitcnt vmcnt(" #N ")" : "+v"(sg[0]), "+v"(sg[1]), "+v"(sg[2]), "+v"(sg[3]), "+v"(sg[4]), "+v"(sg[5]), "+v"(sg[6]), "+v"(sg[7]) :: "memory")
  auto expect = [&](int Tt) {
    const u32x2 lo = (sg[0] + sg[1]) + (sg[2] + sg[3]), hi = (sg[4] + sg[5]) + (sg[6] + sg[7]);
    const u32x2 v = (Tt & 2) ? hi : lo;
    return ((Tt & 1) ? v.y : v.x) - 4u * tag;
  };
  auto redo = [&](int k, f32x4 (&o)[4]) {
    const u32 base = hoff(k);
#pragma unroll 1
    for (u32 spins = 0; spins < SPIN_CAP; ++spins) {
#pragma unroll
      for (int q = 0; q < 8; ++q) loadq(hf[0][q], base, q);
      loadsg();
      WAITSG(0);
      asm volatile("" : "+v"(hf[0][0]), "+v"(hf[0][1]), "+v"(hf[0][2]), "+v"(hf[0][3]), "+v"(hf[0][4]), "+v"(hf[0][5]), "+v"(hf[0][6]), "+v"(hf[0][7]) :: "memory");
      u32 m = 0;
#pragma unroll
      for (int q = 0; q < 8; ++q) m = sum4(m, hf[0][q]);
      if (__all(m == expect((w + 1 + k) & 3))) break;
      __builtin_amdgcn_s_sleep(4);
    }
#pragma unroll
    for (int q = 0; q < 8; ++q)
#pragma unroll
      for (int jt = 0; jt < 4; ++jt) o[jt] = mfma16(asb(hf[0][q]), (q & 1) ? cim[q >> 1][jt] : cre[q >> 1][jt], o[jt]);
  };
  u32 msum = 0, mown = 0, exo;
#pragma unroll
  for (int k = 0; k < 4; ++k) {
    const int hs = k & 1;
#pragma unroll
    for (int jt = 0; jt < 4; ++jt) y[hs][jt] = (k < 3) ? f32x4{0, 0, 0, 0} : skipinit(jt);
#pragma unroll
    for (int q = 0; q < 8; ++q) {
      asm volatile("s_waitcnt vmcnt(%1)" : "+v"(hf[hs][q]) : "i"(k < 2 ? 15 : k == 2 ? 15 - q + (q > 3 ? 8 : 0) : 15 - q) : "memory");
      if (k == 0 && q == 0) { ST(5); }
      if (q == 0) FST(30 + k);
      if (8 * k + q == YB_AT) BAR();
#pragma unroll
      for (int jt = 0; jt < 4; ++jt) {
        y[hs][jt] = mfma16(asb(hf[hs][q]), (q & 1) ? cim[q >> 1][jt] : cre[q >> 1][jt], y[hs][jt]);
        if (jt == 0) { SCHED(); if (k < 3) { msum = sum4(msum, hf[hs][q]); PIN(msum); } else { mown = sum4(mown, hf[hs][q]); PIN(mown); } SCHED(); }
        if (k > 0 && k < 3 && jt == 1 && q >= 1 && q <= 4) { SCHED(); park(k - 1, q - 1, y[hs ^ 1][q - 1]); SCHED(); }
        if (k == 3 && (jt & 1) && q < 2) { SCHED(); park(2, 2 * q + (jt >> 1), y[0][2 * q + (jt >> 1)]); SCHED(); }
      }
      SCHED();
      if (k < 2) loadq(hf[hs][q], hoff(k + 2), q);
      if (k == 2 && q == 3) loadsg();
      if (k == 3 && q == B11_Q) {
        WAITSG(0);
        const u32x2 t2 = ((sg[0] + sg[1]) + (sg[2] + sg[3])) + ((sg[4] + sg[5]) + (sg[6] + sg[7]));
        exo = expect(w);
        if (UNLIKELY(!__all(msum + exo == t2.x + t2.y - 16u * tag))) {
#pragma unroll 1
          for (int kk = 0; kk < 3; ++kk) {
            f32x4 o[4] = {f32x4{0, 0, 0, 0}, f32x4{0, 0, 0, 0}, f32x4{0, 0, 0, 0}, f32x4{0, 0, 0, 0}};
            redo(kk, o);
#pragma unroll
            for (int jt = 0; jt < 4; ++jt) park(kk, jt, o[jt]);
          }
        }
        LBAR();
#pragma unroll
        for (int kk = 0; kk < 3; ++kk)
#pragma unroll
          for (int jt = 0; jt < 4; ++jt) pt[kk][jt] = *(const f32x4*)(smem + L_CI + ((w - 1 - kk) & 3) * 16384 + kk * 4096 + jt * 1024 + lane * 16);
      }
      SCHED();
    }
  }
#pragma unroll
  for (int jt = 0; jt < 4; ++jt)
#pragma unroll
    for (int i = 0; i < 4; ++i) {
      float t;
      asm("v_add_f32 %0, %1, %2\n\tv_add_f32 %0, %0, %3" : "=&v"(t) : "v"(pt[0][jt][i]), "v"(pt[1][jt][i]), "v"(pt[2][jt][i]));
      y0[jt][i] = t;
    }
  if (UNLIKELY(!__all(mown == exo))) {
#pragma unroll
    for (int jt = 0; jt < 4; ++jt) y[1][jt] = skipinit(jt);
    redo(3, y[1]);
  }
  FST(34);
  ST(6);
  asm volatile("s_nop 7" : "+v"(y[1][0]), "+v"(y[1][1]), "+v"(y[1][2]), "+v"(y[1][3]));
  {
    const rsrc_t rsO = mkrs(pr.out, 8u << 20);
    const u32 oo = (u32)row0 * 4096u + (u32)col0 * 4u;
#pragma unroll
    for (int k = 0; k < 4; ++k) {
      f32x4 v;
      asm("v_add_f32 %0, %1, %2" : "=v"(v[0]) : "v"(y[1][0][k]), "v"(y0[0][k]));
      asm("v_add_f32 %0, %1, %2" : "=v"(v[1]) : "v"(y[1][1][k]), "v"(y0[1][k]));
      asm("v_add_f32 %0, %1, %2" : "=v"(v[2]) : "v"(y[1][2][k]), "v"(y0[2][k]));
      asm("v_add_f32 %0, %1, %2" : "=v"(v[3]) : "v"(y[1][3][k]), "v"(y0[3][k]));
      __builtin_amdgcn_raw_buffer_store_b128(__builtin_bit_cast(u32x4, v), rsO, oo + k * 4096u, 0, OUT_A);
    }
  }
  FST(35);
  FDUMP(36);
  if (r == 15 && s == 0 && tid == 0) __builtin_amdgcn_raw_buffer_store_b32(tag, rs, EPA_OFF, 0, AUX_SC1);
  ST(7);
}

DEV void partB(const Params& pr, char* smem, const u32 lds0, u32 tag, u32 ldt2) {
  char* ws = pr.ws;
  const int b = blockIdx.x;
  const int r = 2 * (b & 7) + (b >> 7), s = (b >> 3) & 15;
  const int tid = threadIdx.x & 255, lane = tid & 63;
  const int w = __builtin_amdgcn_readfirstlane(tid >> 6);
  const int c = lane & 15, g = lane >> 4;
  const int ch = w >> 1, half = w & 1, n = tid & 127;
  ST(0);
  FST(0);
  auto udma = [&]() {
    const u32 vo = (u32)(lane >> 2) * 8192u + (u32)((lane & 3) ^ ((lane >> 3) & 3)) * 16u;
    SETM0(lds0 + L_UT + w * 2048);
    const u32x4 rsU = rs4of(pr.u, 2u << 20);
    DMA(vo, rsU, (u32)(128 * s + 2 * w) * 1024u + r * 64u, 0); DMA(vo, rsU, (u32)(128 * s + 2 * w) * 1024u + r * 64u, 1024);
  };
  if (!ULATE) udma();
  PRIO(0);
  const rsrc_t rs = mkrs(ws, WS_USED);
  const u32x4 rs4 = rs4of(ws, WS_USED);
  u32 ga[14];
  {
    const u32 tabw = lds0 + L_TAB + ch * 2 * B_TW;
    auto adr = [&](u32 i) { u32 a = tabw + 4u * (i + (i >> 4)); PIN(a); return a; };
    const int v = 255 - n;
    ga[11] = adr(384 + 2 * v + 2); ga[12] = adr(384 + 2 * v); ga[13] = adr(384 + 2 * v - (n == 127));
    const int q = 127 - n;
    float jb = (float)(8 * q + 7);
    PIN(jb);
#pragma unroll
    for (int part = 0; part < 2; ++part) {
      const int qq = q - part;
      const int e = (qq >= 32) + (qq >= 64);
      const float M = __uint_as_float(0x4B400000u + ((u32)e << 23));
      const u32 mul = 1u + (e & 1), add = (e == 2 ? 384u : 0u) - (__float_as_uint(M) & 0xffffffu) * mul;
#pragma unroll
      for (int k = part * 8; k < (part ? 11 : 8); ++k) {
        float j = jb - (float)k;
        if (part) j = fmaxf(j, 0.f);
        ga[k] = adr((__float_as_uint(j + M) & 0xffffffu) * mul + add);
      }
    }
  }
  SCHED();
  FST(1);
  BAR();
  FST(2);
  asm volatile("s_waitcnt vmcnt(0) lgkmcnt(0)" : "+s"(tag), "+s"(ldt2) :: "memory");
  u32 praw[6];
  {
    const u16* rw = (const u16*)(smem + L_BP + w * 1024) + lane;
#pragma unroll
    for (int k = 0; k < 6; ++k) praw[k] = rw[64 * k];
  }
  SCHED();
  float bzr, bzi, unscale;
  c1 W;
  {
    const float dt = rbf(__builtin_amdgcn_exp2f((ch ? bfhi(ldt2) : bf2f(ldt2)) * LOG2E));
    bzr = dt * bf2f(praw[0]) * LOG2E;
    bzi = dt * bf2f(praw[1]) * INV2PI;
    bzi -= __builtin_rintf(bzi);
    W = cmul1(c1{bf2f(praw[4]), bf2f(praw[5])}, c1{dt * bf2f(praw[2]), dt * bf2f(praw[3])});
    const u32 sf = min(264u - (wave_max(max(__float_as_uint(W.r) & 0x7fffffffu, __float_as_uint(W.i) & 0x7fffffffu)) >> 23), 253u) << 23;
    unscale = __uint_as_float(0x7f000000u - sf);
    W.r *= __uint_as_float(sf); PIN(W.r); W.i *= __uint_as_float(sf); PIN(W.i); PIN(unscale); PIN(bzr); PIN(bzi);
  }
  SCHED();
  BAR();
  u32 tb[28];
  c1 ea = cexp1(bzr, bzi, 1.f, false), eb = cexp1(bzr, bzi, 16.f, false), e2, e64;
  level_a(ea, e2, tb[0], tb[1]); level_b<true>(ea, e2, tb[2], ea);
  level_a(eb, e2, tb[3], tb[4]); level_b<true>(eb, e2, tb[5], e64);
  SCHED();
  BAR();
  level_a(ea, e2, tb[6], tb[7]); level_b<false>(ea, e2, tb[8], ea);
  level_a(e64, e2, tb[9], tb[10]); level_b<false>(e64, e2, tb[11], ea);
  const c1 e256 = cexp1(bzr, bzi, 256.f, false);
  c1 V = cmul1(W, csq1(e256));
  SCHED();
  BAR();
#pragma unroll
  for (int m = 0; m < 4; ++m) wstep(W, V, e64, e256, tb[12 + 2 * m], tb[13 + 2 * m]);
  SCHED();
  WAIT_V(0);
  BAR();
  u32x4 uv0, uv1;
  auto utr = [&]() {
    const u32* tp = (const u32*)(smem + L_UT + c * 64 + ((w ^ ((c >> 1) & 3)) * 16) + g * 4);
    u32 tu[8];
#pragma unroll
    for (int e = 0; e < 8; ++e) tu[e] = tp[e * 256];
    uv0 = u32x4{__builtin_amdgcn_perm(tu[1], tu[0], 0x05040100u), __builtin_amdgcn_perm(tu[3], tu[2], 0x05040100u),
                __builtin_amdgcn_perm(tu[5], tu[4], 0x05040100u), __builtin_amdgcn_perm(tu[7], tu[6], 0x05040100u)};
    uv1 = u32x4{__builtin_amdgcn_perm(tu[1], tu[0], 0x07060302u), __builtin_amdgcn_perm(tu[3], tu[2], 0x07060302u),
                __builtin_amdgcn_perm(tu[5], tu[4], 0x07060302u), __builtin_amdgcn_perm(tu[7], tu[6], 0x07060302u)};
    PIN(uv0); PIN(uv1);
  };
  if (!ULATE) utr();
#pragma unroll
  for (int m = 4; m < 6; ++m) wstep(W, V, e64, e256, tb[12 + 2 * m], tb[13 + 2 * m]);
  asm volatile("" : "+s"(tag)); tag = next_tag(tag); asm volatile("" : "+s"(tag));
  SCHED();
  BAR();
  FST(3);
  wstep(W, V, e64, e256, tb[24], tb[25]);
  tb[26] = pkh(W); tb[27] = pkh(V);
  SCHED();
  if (ULATE) udma();
  u32 wa[4];
#pragma unroll
  for (int j = 0; j < 4; ++j) { wa[j] = lds0 + L_CI + w * 16384 + j * 256 + ((opq(lane) * 4) ^ (j * 64)); PIN(wa[j]); }
  SCHED();
  FST(4);
  BAR();
  FST(5);
  ST(1);
  u32 sink;
  {
    const u32x4 rsC = rs4of((w & 1) ? pr.ACim : pr.ACre, 512u << 10);
    asm volatile("buffer_load_dword %0, %1, %2, %3 offen" : "=v"(sink) : "v"(lane * 128u), "s"(rsC), "s"((u32)((b >> 3) * 2 + (w >> 1)) * 8192u) : "memory");
  }
  auto upub = [&]() {
    const u32 uo = UT_OFF + (u32)(32 * r + 2 * (tid >> 4)) * 4096u + s * 256u + c * 16u;
    st128(uv0, rs, uo); st128(uv1, rs, uo + 4096u);
    __builtin_amdgcn_raw_buffer_store_b32(sum4(sum4(tag, uv0), uv1), rs, US_OFF + (u32)((r * 16 + s) * 256 + tid) * 4u, 0, AUX_SC1);
  };
  if (!ULATE) upub();
  SCHED();
  {
    auto put2 = [&](int row, int row2, u32 v, u32 v2) {
      asm volatile("ds_write2st64_b32 %0, %1, %2 offset0:%3 offset1:%4" :: "v"(wa[row & 3]), "v"(v), "v"(v2), "i"(row & ~3), "i"(row2 & ~3) : "memory");
    };
    u32 one = 0x00003C00u;
    PIN(one);
    if (w == 0) *(u32*)(smem + L_SM + lane * 4) = 0u;
    put2(0, 4, one, one); put2(8, 12, one, one);
#pragma unroll
    for (int i = 0; i < 3; ++i) { put2(1 + i, 9 + i, tb[i], tb[3 + i]); put2(5 + i, 13 + i, tb[6 + i], tb[9 + i]); }
#pragma unroll
    for (int m = 0; m < 8; ++m) put2(16 + m, 24 + m, tb[12 + 2 * m], tb[13 + 2 * m]);
  }
  u32x4 uc[2];
  u32 sg1;
  auto fetch = [&](u32 off, u32 step, u32 soff) {
    asm volatile("buffer_load_dwordx4 %0, %3, %5, 0 offen sc1\n\tbuffer_load_dwordx4 %1, %3, %5, %6 offen sc1\n\tbuffer_load_dword %2, %4, %5, 0 offen sc1"
                 : "=&v"(uc[0]), "=&v"(uc[1]), "=&v"(sg1) : "v"(off), "v"(soff), "s"(rs4), "s"(step) : "memory");
  };
#define WAITF(N) asm volatile("s_waitcnt vmcnt(%3)" : "+v"(uc[0]), "+v"(uc[1]), "+v"(sg1) : "i"(N) : "memory")
  auto whole = [&]() { return __all(sum4(sum4(tag, uc[0]), uc[1]) == sg1); };
  const u32 uoff = UT_OFF + (u32)(32 * r + 2 * s) * 4096u + tid * 16u;
  const u32 usoff = US_OFF + (u32)((r * 16 + (tid >> 4)) * 256 + s * 16 + c) * 4u;
  {
    f32x4 T[4];
#pragma unroll
    for (int k = 0; k < 4; ++k) T[k] = f32x4{0, 0, 0, 0};
    const u32 tb0 = lds0 + L_CI + w * 16384 + g * 16;
    const u32 ta = tb0 + (c >> 2) * 320, tbb = tb0 + (c & 3) * 320;
    u32x4 qa[2][6], qb[2][4];
    auto rda = [&](int st, int i, int off) { qa[st & 1][i] = *(const LAS u32x4*)(size_t)((ta ^ (st * 64)) + off); };
    auto rdb = [&](int st, int i) { qb[st & 1][i] = *(const LAS u32x4*)(size_t)((tbb ^ (st * 64)) + i * 1024); };
    auto rdq = [&](int st, int part) {
      if (st < 4) {
        if (part == 0) { rda(st, 0, 1024); rdb(st, 0); }
        if (part == 1) { rda(st, 2, 4096); rdb(st, 2); }
        if (part == 2) { rda(st, 3, 5120); rda(st, 1, 2048); }
        if (part == 3) { rdb(st, 1); rda(st, 4, 6144); }
        if (part == 4) { rdb(st, 3); rda(st, 5, 7168); }
      }
      SCHED();
    };
#pragma unroll
    for (int i = 0; i < 5; ++i) rdq(0, i);
#pragma unroll
    for (int st = 0; st < 4; ++st) {
      const int z = st & 1;
      CWAIT_L0();
      if (st == 0) FST(21);
      if (st == B7_AT) { FST(6); if (ULATE) WAIT_V(1); BAR(); PRIO(2); FST(7); if (ULATE) { utr(); upub(); CWAIT_L0(); } }
      SCHED();
      const f16x8 F0 = cmulh4<false>(qa[z][0], qb[z][0]);
      SCHED(); rdq(st + 1, 0);
      const f16x8 Ga = cmulh4<true>(qa[z][2], qb[z][2]);
      SCHED(); rdq(st + 1, 1);
      const f16x8 Gb = cmulh4<true>(qa[z][3], qb[z][2]);
      SCHED(); rdq(st + 1, 2);
      T[0] = mfma16h(F0, Ga, T[0]);
      SCHED();
      const f16x8 F2 = cmulh4<false>(qa[z][1], qb[z][1]);
      SCHED(); rdq(st + 1, 3);
      T[1] = mfma16h(F0, Gb, T[1]);
      SCHED();
      const f16x8 Gc = cmulh4<true>(qa[z][4], qb[z][3]);
      SCHED(); rdq(st + 1, 4);
      f16x8 Gd = cmulh4<true>(qa[z][5], qb[z][3]);
      SCHED();
      T[2] = mfma16h(F2, Gc, T[2]);
      asm volatile("s_nop 0" : "+v"(Gd));
      T[3] = mfma16h(F2, Gd, T[3]);
      SCHED();
    }
    FST(8);
    if (!ULATE) { PRIO(3); fetch(uoff, 4096u, usoff); PRIO(2); }
    const u32 tw = lds0 + L_TAB + w * B_TW + (17 * c + 4 * g) * 4;
#pragma unroll
    for (int k = 0; k < 4; ++k) {
      const f32x4 t = T[k] * unscale;
      const u32 ad = tw + k * 1088;
      asm volatile("ds_write2_b32 %0, %1, %2 offset1:1\n\tds_write2_b32 %0, %3, %4 offset0:2 offset1:3" :: "v"(ad), "v"(t[0]), "v"(t[1]), "v"(t[2]), "v"(t[3]) : "memory");
    }
  }
  if (ULATE) fetch(uoff, 4096u, usoff);
  WAIT_L0();
  FST(9);
  BAR();
  FST(10);
  ST(2);
  {
    float v[14], t0[14], t1[14];
#pragma unroll
    for (int k = 13; k >= 0; --k) {
      const LAS float* t = (const LAS float*)(size_t)ga[k];
      t0[k] = t[0]; t1[k] = t[1088];
    }
    if (w == 0) *(u32x4*)(smem + L_U + (lane >> 5) * 4608 + (lane & 31) * 16) = u32x4{0, 0, 0, 0};
    if (w == 1) *(u32x4*)(smem + L_R + (c >> 1) * B_CS + 4096 + (lane & 1) * 16) = u32x4{0, 0, 0, 0};
    SCHED();
#pragma unroll
    for (int k = 13; k >= 0; --k) asm("v_add_f32 %0, %1, %2" : "=v"(v[k]) : "v"(t0[k]), "v"(t1[k]));
    char* rb = smem + L_R + ch * 4 * B_CS + n * 16;
    const float hi = v[11], lo = v[12], ti = (n & 1) ? lo : hi;
    const u32 hh2 = pk2(hi, hi), ht = pk2(hi, ti), tl = pk2(ti, lo), ll = pk2(lo, lo), ls = pk2(lo, v[13]);
    *(u32x4*)(rb) = u32x4{hh2, ht, ll, ll};
    *(u32x4*)(rb + B_CS) = u32x4{hh2, tl, ll, ll};
    *(u32x4*)(rb + 2 * B_CS) = u32x4{ht, ll, ll, ll};
    *(u32x4*)(rb + 3 * B_CS) = u32x4{tl, ll, ll, ls};
    if (n == 127) { v[8] = 0.f; v[9] = 0.f; v[10] = 0.f; }
#pragma unroll
    for (int sg = 0; sg < 4; ++sg)
      *(u32x4*)(rb + 2048 + sg * B_CS) = u32x4{pk2(v[sg], v[sg + 1]), pk2(v[sg + 2], v[sg + 3]), pk2(v[sg + 4], v[sg + 5]), pk2(v[sg + 6], v[sg + 7])};
  }
  SCHED();
  FST(11);
  WAITF(0);
  PIN(sink);
  if (UNLIKELY(!whole())) {
#pragma unroll 1
    for (u32 spins = 0; spins < SPIN_CAP; ++spins) {
      __builtin_amdgcn_s_sleep(2);
      fetch(uoff, 4096u, usoff);
      WAITF(0);
      if (whole()) break;
    }
  }
  *(u32x4*)(smem + L_U + 512 + tid * 16) = uc[0];
  *(u32x4*)(smem + L_U + 512 + 4608 + tid * 16) = uc[1];
  FST(12);
  LBAR();
  PRIO(0);
  FST(13);
  ST(3);
  const int cq = tid & 7, rp = tid >> 3;
  const int lb0 = 1024 * (s >> 3) + 128 * (s & 1) + 4 * ((s >> 1) & 3) + 256 * (rp >> 3) + 16 * (rp & 7);
  u32x2 ut[4], dsk;
  f32x4 acc[8];
  {
    const int beta = 15 - c;
    const u32 abase = lds0 + L_R + ch * 4 * B_CS + (beta & 3) * B_CS + 16 * g + 2 * (beta & 12) + 2 * (32 - 32 * half);
    const u32 ubase = lds0 + L_U + ch * 4608 + 512 + 2 * (-16 - 32 * half + 16 * c + 8 * g) - 384;
    u32x2 alo[2][8], ahi[2][8];
    u32x4 bfr[2][8];
    auto rd1 = [&](int rr, int i) {
      const int z = rr & 1;
      if (i < 8) asm volatile("ds_read_b128 %0, %1 offset:%2" : "=v"(bfr[z][i & 7]) : "v"(ubase), "i"(512 * (i & 7) + 128 * (3 - rr)) : "memory");
      else if (i & 1) asm volatile("ds_read_b64 %0, %1 offset:%2" : "=v"(ahi[z][((i - 8) >> 1) & 7]) : "v"(abase), "i"(512 * (7 - (((i - 8) >> 1) & 7)) + 128 * (3 - rr) + 8) : "memory");
      else asm volatile("ds_read_b64 %0, %1 offset:%2" : "=v"(alo[z][((i - 8) >> 1) & 7]) : "v"(abase), "i"(512 * (7 - (((i - 8) >> 1) & 7)) + 128 * (3 - rr)) : "memory");
    };
#pragma unroll
    for (int i = 0; i < 24; ++i) rd1(0, i);
#pragma unroll
    for (int t = 0; t < 8; ++t) acc[t] = f32x4{0, 0, 0, 0};
#pragma unroll
    for (int rr = 0; rr < 4; ++rr) {
      const int z = rr & 1;
      WAIT_L0();
      SCHED();
#pragma unroll
      for (int mu = 0; mu < 8; ++mu)
#pragma unroll
        for (int t = mu; t < 8; ++t) {
          const int idx = mu * 8 - mu * (mu - 1) / 2 + (t - mu);
          acc[t] = mfma16(asb(u32x4{alo[z][mu].x, alo[z][mu].y, ahi[z][mu].x, ahi[z][mu].y}), asb(bfr[z][t - mu]), acc[t]);
          SCHED();
          if (rr < 3 && idx >= 2 && idx < 26) { rd1(rr + 1, idx - 2); SCHED(); }
        }
    }
  }
  PRIO(3);
  FST(14);
  ST(4);
  {
    char* xb = smem + L_TAB + w * 4096 + lane * 16;
    const char* ob = smem + L_TAB + (w ^ 1) * 4096 + lane * 16;
    const int d = 32 * r + 2 * s + ch;
    const u32 yo = YT_OFF + (u32)d * 4096u + half * 2048u + lane * 16u;
    auto fin = [&](int keep) {
#pragma unroll
      for (int t = 0; t < 4; ++t) *(f32x4*)(xb + t * 1024) = acc[(keep ^ 4) + t];
      LBAR();
      f32x4 o[4];
#pragma unroll
      for (int t = 0; t < 4; ++t) o[t] = *(const f32x4*)(ob + t * 1024);
      SCHED();
      u32 pk[8];
#pragma unroll
      for (int t = 0; t < 4; ++t) {
        float a[4];
#pragma unroll
        for (int i = 0; i < 4; ++i) asm("v_add_f32 %0, %1, %2" : "=v"(a[i]) : "v"(o[t][i]), "v"(acc[keep + t][i]));
        pk[2 * t] = pk2(a[0], a[1]); pk[2 * t + 1] = pk2(a[2], a[3]);
      }
      const u32x4 v0 = u32x4{pk[0], pk[1], pk[2], pk[3]}, v1 = u32x4{pk[4], pk[5], pk[6], pk[7]};
      st128(v0, rs, yo); st128(v1, rs, yo + 1024u);
      __builtin_amdgcn_raw_buffer_store_b32(sum4(sum4(tag, v0), v1), rs, YS_OFF + (u32)(d * 2 + half) * 256u + lane * 4u, 0, AUX_SC1);
    };
    if (half) { asm volatile("; half 1" ::: "memory"); fin(4); } else { asm volatile("; half 0" ::: "memory"); fin(0); }
  }
  SCHED();
  FST(15);
  ST(5);
    {
      const u32x4 ra = rs4of(pr.u, 2u << 20), rd = rs4of(pr.BD, 1024u);
      asm volatile("buffer_load_dwordx2 %0, %5, %6, 0 offen\n\tbuffer_load_dwordx2 %1, %5, %6, 0 offen offset:1024\n\t"
                   "buffer_load_dwordx2 %2, %5, %6, 0 offen offset:2048\n\tbuffer_load_dwordx2 %3, %5, %6, 0 offen offset:3072\n\t"
                   "buffer_load_dwordx2 %4, %7, %8, 0 offen"
                   : "=&v"(ut[0]), "=&v"(ut[1]), "=&v"(ut[2]), "=&v"(ut[3]), "=&v"(dsk)
                   : "v"((u32)lb0 * 1024u + r * 64u + cq * 8u), "s"(ra), "v"(r * 64u + cq * 8u), "s"(rd) : "memory");
    }
  PRIO(0);
  asm volatile("s_waitcnt vmcnt(0)" : "+v"(ut[0]), "+v"(ut[1]), "+v"(ut[2]), "+v"(ut[3]), "+v"(dsk) :: "memory");
  {
    const float d0 = bf2f(dsk.x), d1 = bfhi(dsk.x), d2 = bf2f(dsk.y), d3 = bfhi(dsk.y);
#pragma unroll
    for (int k = 0; k < 4; ++k) {
      float m[4] = {bf2f(ut[k].x) * d0, bfhi(ut[k].x) * d1, bf2f(ut[k].y) * d2, bfhi(ut[k].y) * d3};
      ut[k] = u32x2{pk2(m[0], m[1]), pk2(m[2], m[3])};
      PIN(ut[k]);
    }
  }
  const u32 ln = ((s >> 1) & 3) * 16 + (s & 1) * 8 + cq;
  const u32 yoff = YT_OFF + (u32)(32 * r + rp) * 4096u + (u32)(s >> 3) * 2048u + ln * 16u;
  const u32 ysoff = YS_OFF + (u32)((32 * r + rp) * 2 + (s >> 3)) * 256u + ln * 4u;
  SCHED();
  BAR();
  FST(16);
  PRIO(3);
#pragma unroll 1
  for (u32 spins = 0; spins < SPIN_CAP; ++spins) {
    fetch(yoff, 1024u, ysoff);
    WAITF(0);
    if (whole()) break;
    __builtin_amdgcn_s_sleep(4);
  }
  FST(17);
  ST(6);
  {
    char* tbp = smem + L_YT + rp * B_TS + cq * 8;
    *(u32x2*)(tbp) = u32x2{uc[0].x, uc[0].y}; *(u32x2*)(tbp + 64) = u32x2{uc[0].z, uc[0].w};
    *(u32x2*)(tbp + 128) = u32x2{uc[1].x, uc[1].y}; *(u32x2*)(tbp + 192) = u32x2{uc[1].z, uc[1].w};
  }
  LBAR();
  {
    const rsrc_t rsO = mkrs(pr.out, 8u << 20);
    const char* tbp = smem + L_YT + (4 * cq) * B_TS + rp * 8;
    const u32x2 y0 = *(const u32x2*)(tbp), y1 = *(const u32x2*)(tbp + B_TS), y2 = *(const u32x2*)(tbp + 2 * B_TS), y3 = *(const u32x2*)(tbp + 3 * B_TS);
    const u32 oo = (u32)lb0 * 4096u + 2048u + r * 128u + cq * 16u;
#pragma unroll
    for (int k = 0; k < 4; ++k) {
      const u32 q0 = (k & 2) ? y0.y : y0.x, q1 = (k & 2) ? y1.y : y1.x, q2 = (k & 2) ? y2.y : y2.x, q3 = (k & 2) ? y3.y : y3.x;
      float a[4];
      asm("v_add_f32 %0, %1, %2" : "=v"(a[0]) : "v"((k & 1) ? bfhi(q0) : bf2f(q0)), "v"(bf2f(ut[k].x)));
      asm("v_add_f32 %0, %1, %2" : "=v"(a[1]) : "v"((k & 1) ? bfhi(q1) : bf2f(q1)), "v"(bfhi(ut[k].x)));
      asm("v_add_f32 %0, %1, %2" : "=v"(a[2]) : "v"((k & 1) ? bfhi(q2) : bf2f(q2)), "v"(bf2f(ut[k].y)));
      asm("v_add_f32 %0, %1, %2" : "=v"(a[3]) : "v"((k & 1) ? bfhi(q3) : bf2f(q3)), "v"(bfhi(ut[k].y)));
      const u32 r01 = pk2(a[0], a[1]), r23 = pk2(a[2], a[3]);
      __builtin_amdgcn_raw_buffer_store_b128(u32x4{r01 << 16, r01 & 0xffff0000u, r23 << 16, r23 & 0xffff0000u}, rsO, oo + k * 4096u, 0, OUT_B);
    }
  }
  FST(18);
  FDUMP(22);
  ST(7);
}

__global__ __launch_bounds__(512, 2) void ssm_fused_kernel(Params pr) {
  extern __shared__ __attribute__((aligned(16))) char smem[];
  const u32 lds0 = (u32)(size_t)(LAS char*)smem;
  if (__builtin_amdgcn_readfirstlane(threadIdx.x) == 448) {
    u64 pc;
    asm volatile("s_getpc_b64 %0" : "=s"(pc));
    pc &= ~255ull;
    const u32 line = min((blockIdx.x & ~7u) + (threadIdx.x & 7), (u32)CODE_LINES - 1);
    asm volatile("s_mov_b32 m0, %2\n\ts_nop 0\n\tglobal_load_lds_dword %0, %1" :: "v"(line * 128u), "s"(pc), "s"(lds0 + L_PF) : "memory");
  }
  u32 tagB = 0, ldt2 = 0;
  const bool roleB = __builtin_amdgcn_readfirstlane(threadIdx.x) >= 256;
  if (roleB) {
    const int b = blockIdx.x, r = 2 * (b & 7) + (b >> 7), s = (b >> 3) & 15, lane = threadIdx.x & 63;
    const int w = __builtin_amdgcn_readfirstlane(threadIdx.x >> 6) & 3;
    __builtin_amdgcn_s_setprio(3);
    PINARGS();
    {
      u64 p0 = (u64)pr.Are, p1 = (u64)pr.Aim, p2 = (u64)pr.BBre, p3 = (u64)pr.BBim, p4 = (u64)pr.BCre, p5 = (u64)pr.BCim;
      asm volatile("" : "+s"(p0), "+s"(p1), "+s"(p2), "+s"(p3), "+s"(p4), "+s"(p5));
      u64 pa = (lane & 8) ? p1 : p0, pb = (lane & 8) ? p3 : p2, pc = (lane & 8) ? p5 : p4;
      PIN(pa); PIN(pb); PIN(pc);
      const u64 src = (lane & 32 ? pc : (lane & 16) ? pb : pa) + (u32)((32 * r + 2 * s) * 256 + w * 128 + (lane & 7) * 16);
      asm volatile("s_mov_b32 m0, %1\n\ts_nop 0\n\tglobal_load_lds_dwordx4 %0, off" :: "v"(src), "s"(lds0 + L_BP + w * 1024) : "memory");
    }

  {
    u64 q0 = (u64)pr.Lre, q1 = (u64)pr.Lim, q2 = (u64)pr.Alogdt;
    asm volatile("" : "+s"(q0), "+s"(q1), "+s"(q2));
    const u64 srcA = (((lane >> 4) == 0) ? q0 : ((lane >> 4) == 1) ? q1 : q2) + (u32)(64 * s + 4 * (lane & 15));
    asm volatile("s_mov_b32 m0, %1\n\ts_nop 0\n\tglobal_load_lds_dword %0, off" :: "v"(srcA), "s"(lds0 + L_AP + w * 256) : "memory");
  }
    asm volatile("s_load_dword %0, %2, %4\n\ts_load_dword %1, %3, %5" : "=&s"(tagB), "=&s"(ldt2)
                 : "s"(pr.ws), "s"(pr.Blogdt), "s"(EPA_OFF), "s"((u32)(64 * r + 4 * s)) : "memory");
    {
      const void* t0 = w == 0 ? (const void*)pr.ws : w == 1 ? (const void*)pr.out : w == 2 ? (const void*)pr.u : (const void*)pr.AD;
      const void* t1 = w == 0 ? (const void*)pr.BD : w == 1 ? (const void*)pr.BD : w == 2 ? (const void*)pr.ACre : (const void*)pr.ACim;
      const u32 n0 = w == 0 ? WS_USED : w == 1 ? 8u << 20 : w == 2 ? 2u << 20 : 1024u;
      SETM0(lds0 + L_PF);
      asm volatile("buffer_load_dword %0, %1, 0 offen lds\n\tbuffer_load_dword %3, %2, 0 offen lds"
                   :: "v"((u32)(lane & 15) << 20), "s"(rs4of(t0, n0)), "s"(rs4of(t1, 1024u)), "v"(0u) : "memory");
    }
  }
  asm volatile("" ::: "memory");
  if (UNLIKELY(roleB)) partB(pr, smem, lds0, tagB, ldt2);
  else partA(pr, smem, lds0);
}

extern "C" void kernel_launch(void* const* d_in, const int* in_sizes, int n_in, void* d_out, int out_size,
                              void* d_ws, size_t ws_size, hipStream_t stream) {
  Params p;
  p.x = (const u16*)d_in[0]; p.Lre = (const u16*)d_in[1]; p.Lim = (const u16*)d_in[2];
  p.ABre = (const u16*)d_in[3]; p.ABim = (const u16*)d_in[4]; p.ACre = (const u16*)d_in[5]; p.ACim = (const u16*)d_in[6];
  p.Alogdt = (const u16*)d_in[7]; p.AD = (const u16*)d_in[8];
  p.u = (const u16*)d_in[9]; p.Are = (const u16*)d_in[10]; p.Aim = (const u16*)d_in[11];
  p.BBre = (const u16*)d_in[12]; p.BBim = (const u16*)d_in[13]; p.BCre = (const u16*)d_in[14]; p.BCim = (const u16*)d_in[15];
  p.Blogdt = (const u16*)d_in[16]; p.BD = (const u16*)d_in[17];
  p.out = (float*)d_out; p.ws = (char*)d_ws;
  (void)hipFuncSetAttribute((const void*)ssm_fused_kernel, hipFuncAttributeMaxDynamicSharedMemorySize, LDS_BYTES);
  ssm_fused_kernel<<<256, 512, LDS_BYTES, stream>>>(p);
}
```
